```python
import torch

def _softmax(x, axis=-1):
    return torch.softmax(x, dim=axis)
def _gelu(x):
    return torch.nn.functional.gelu(x, approximate="tanh")
def _silu(x):
    return torch.nn.functional.silu(x)
def _sigmoid(x):
    return torch.sigmoid(x)
def _layernorm(x, axis=-1, eps=1e-5):
    m = x.mean(dim=axis, keepdim=True)
    v = x.var(dim=axis, keepdim=True, unbiased=False)
    return (x - m) / torch.sqrt(v + eps)
def _scatter_add(tgt, idx, src, axis=0):
    return tgt.clone().index_add_(axis, idx, src)
def _segment_sum(src, seg, n):
    out = torch.zeros((n,) + src.shape[1:], dtype=src.dtype)
    return out.index_add_(0, seg, src)
def _pad(x, pw):
    flat = tuple(v for pair in reversed(pw) for v in pair)
    return torch.nn.functional.pad(x, flat)

kernel_name = 'standalone__gqa_softmax__near_ridge'

def setup_inputs():

    _w = (lambda a: (lambda t: t.to(torch.bfloat16) if t.is_floating_point() else t)(torch.as_tensor(a)))
    B, Hq, Hkv, Sq, Sk, D = 16, 32, 4, 48, 1024, 128
    return {
        "q": _w((lambda *s: torch.randn(*s, dtype=torch.bfloat16) * 0.8)(B, Hq, Sq, D)),
        "k": _w((lambda *s: torch.randn(*s, dtype=torch.bfloat16) * 0.8)(B, Hkv, Sk, D)),
        "v": _w((lambda *s: torch.randn(*s, dtype=torch.bfloat16) * 0.05)(B, Hkv, Sk, D)),
    }

def reference(q, k, v):
    B, Hq, Sq, D = q.shape
    Hkv = k.shape[1]
    rep = Hq // Hkv
    k = (lambda x, n, axis: torch.repeat_interleave(x, n, dim=axis))(k, rep, axis=1)
    v = (lambda x, n, axis: torch.repeat_interleave(x, n, dim=axis))(v, rep, axis=1)
    scale = D ** -0.5
    scores = torch.einsum("bhqd,bhkd->bhqk", q, k) * scale
    p = _softmax(scores, axis=-1)
    return torch.einsum("bhqk,bhkd->bhqd", p, v)
```

```cpp
#include <hip/hip_runtime.h>
#include <hip/hip_bf16.h>

#define ATTN_B 16
#define ATTN_HQ 32
#define ATTN_HKV 8
#define ATTN_SQ 48
#define ATTN_SK 1024
#define ATTN_NSPLIT 2
#define ATTN_THR 8
#define ATTN_KVCONV 0
#define ATTN_CAUSAL 0
#define ATTN_LENS 0
#define ATTN_QPARK (4 + ATTN_LENS)
#define ATTN_WG4 1
#define ATTN_QPARK4 4
#define ATTN_XCHG4 1
constexpr int B = ATTN_B, HQ = ATTN_HQ, HKV = ATTN_HKV, SQ = ATTN_SQ, SK = ATTN_SK;
constexpr int NSPLIT = ATTN_NSPLIT;
constexpr float THR = ATTN_THR;
constexpr int CAUSAL = ATTN_CAUSAL;
constexpr int HAS_LENS = ATTN_LENS;
constexpr int KV_CONVERT = ATTN_KVCONV;
constexpr int QPARK = ATTN_QPARK;
constexpr int WG4 = ATTN_WG4;
constexpr int XCHG4 = ATTN_XCHG4;
constexpr int QPARK4 = ATTN_QPARK4;
constexpr int D = 128, NW = 8, QBLK = 32, KVBLK = 64;
constexpr float SCALE = 0.088388347648318447f;
constexpr int G = HQ / HKV, ROWS = G * SQ, NT = SK / KVBLK, TPS = NT / NSPLIT;
constexpr int NWB = (WG4 && ROWS <= QBLK * 4 && !CAUSAL && !HAS_LENS && TPS % 4 == 0) ? 4 : NW, NSB = NSPLIT * (NW / NWB), TPSB = NT / NSB;
static_assert(HQ % HKV == 0 && ROWS >= 1 && ROWS <= QBLK * NW, "G*SQ must fit the workgroup's 256 MFMA rows (larger SQ -> dense body)");
static_assert(!(CAUSAL && HAS_LENS && !KV_CONVERT), "CAUSAL + HAS_LENS on the f32 convert-on-load loop spills 2 VGPRs (ROCm 7.2, QPARK 4..8): set KV_CONVERT 1 (bf16 loop, 0 spill) for that combination");
static_assert(SK % KVBLK == 0 && NT % NSPLIT == 0 && TPS >= 2 && TPS % 2 == 0, "SK must give every split an even number (>= 2) of 64-key tiles");
static_assert(NT % NSB == 0 && TPSB >= 2 && TPSB % 2 == 0, "internal: half-split tile count (NWB falls back to 8 waves when TPS % 4 != 0)");
constexpr float L2E = 1.4426950408889634f, SL2E = SCALE * L2E, THR2 = THR * L2E;
constexpr size_t SHM_V = KVBLK * D * 2, SHM_K = KVBLK * D * 2, SHM_WS = 2 * SHM_V + 2 * SHM_K;
template <int NWT> constexpr size_t shm_qp() { return SHM_WS + ((NWT == NW || !XCHG4) ? (size_t)NWT * 64 * 4 : 0); }
static_assert(QPARK >= 0 && QPARK <= 8 && QPARK4 >= 0 && QPARK4 <= 8, "QPARK");
constexpr size_t WS_PML = 4096, PML_BYTES = (size_t)B * HQ * SQ * NSB * 2 * 4;
constexpr size_t WS_PO = (WS_PML + PML_BYTES + 255) / 256 * 256, WS_PEND = NSB > 1 ? WS_PO + (size_t)B * HQ * SQ * NSB * D * 4 : WS_PML;
constexpr size_t NQ = (size_t)B * HQ * SQ * D, NKV = (size_t)B * HKV * SK * D;
constexpr size_t WS_QB = (WS_PEND + 255) / 256 * 256, WS_KB = WS_QB + NQ * 2, WS_VB = WS_KB + NKV * 2;
constexpr size_t WS_END = KV_CONVERT ? WS_VB + NKV * 2 : WS_PEND;

using bf16 = __hip_bfloat16;
using bf16x8 = __attribute__((ext_vector_type(8))) short;
using s16x4 = __attribute__((ext_vector_type(4))) short;
using f32x16 = __attribute__((ext_vector_type(16))) float;
using f32x4 = __attribute__((ext_vector_type(4))) float;
using u32x4 = __attribute__((ext_vector_type(4))) unsigned;
template <class T> struct InCfg;
template <> struct InCfg<__hip_bfloat16> { static constexpr int NWT = NWB, QP = NWB == 4 ? QPARK4 : 0; };
template <> struct InCfg<float> { static constexpr int NWT = NW, QP = QPARK; };
template <class T> constexpr int nw_for() { return InCfg<T>::NWT; }
template <class T> constexpr int ns_for() { return NSPLIT * (NW / InCfg<T>::NWT); }
template <class T> constexpr size_t shm_bytes() { return shm_qp<InCfg<T>::NWT>() + (size_t)InCfg<T>::QP * 64 * InCfg<T>::NWT * 16; }
#define KSWZ(row, colB) ((row) * 256 + ((colB) ^ (((row) & 7) << 4)))
#define SBAR() __builtin_amdgcn_sched_barrier(0)
__device__ __forceinline__ int crow(int r, int hi) { return (r & 3) + 8 * (r >> 2) + 4 * hi; }
__device__ __forceinline__ unsigned cvtpk(float lo, float hi) { unsigned r; asm volatile("v_cvt_pk_bf16_f32 %0, %1, %2" : "=v"(r) : "v"(lo), "v"(hi)); return r; }
__device__ __forceinline__ bf16x8 pack8(f32x4 a, f32x4 b) { u32x4 w = {cvtpk(a[0], a[1]), cvtpk(a[2], a[3]), cvtpk(b[0], b[1]), cvtpk(b[2], b[3])}; return *reinterpret_cast<bf16x8*>(&w); }
__device__ __forceinline__ bf16x8 ld8(const bf16* p) { return *reinterpret_cast<const bf16x8*>(p); }
__device__ __forceinline__ bf16x8 ld8(const float* p) { return pack8(*reinterpret_cast<const f32x4*>(p), *reinterpret_cast<const f32x4*>(p + 4)); }
template <class T, int NR> struct Stage;
template <> struct Stage<bf16, 2> { bf16x8 v0, v1, k0, k1;
  __device__ __forceinline__ void load(const bf16* V0, const bf16* V1, const bf16* K0, const bf16* K1) { v0 = ld8(V0); v1 = ld8(V1); k0 = ld8(K0); k1 = ld8(K1); }
  __device__ __forceinline__ bf16x8 gv0() const { return v0; } __device__ __forceinline__ bf16x8 gv1() const { return v1; }
  __device__ __forceinline__ bf16x8 gk0() const { return k0; } __device__ __forceinline__ bf16x8 gk1() const { return k1; } };
template <> struct Stage<float, 2> { f32x4 v0a, v0b, v1a, v1b, k0a, k0b, k1a, k1b;
  __device__ __forceinline__ void load(const float* V0, const float* V1, const float* K0, const float* K1) {
    v0a = *(const f32x4*)V0; v0b = *(const f32x4*)(V0 + 4); v1a = *(const f32x4*)V1; v1b = *(const f32x4*)(V1 + 4);
    k0a = *(const f32x4*)K0; k0b = *(const f32x4*)(K0 + 4); k1a = *(const f32x4*)K1; k1b = *(const f32x4*)(K1 + 4); }
  __device__ __forceinline__ bf16x8 gv0() const { return pack8(v0a, v0b); } __device__ __forceinline__ bf16x8 gv1() const { return pack8(v1a, v1b); }
  __device__ __forceinline__ bf16x8 gk0() const { return pack8(k0a, k0b); } __device__ __forceinline__ bf16x8 gk1() const { return pack8(k1a, k1b); } };
template <int NR> struct Stage<bf16, NR> { bf16x8 v[NR], k[NR];
  __device__ __forceinline__ void ldv(int i, const bf16* p) { v[i] = ld8(p); } __device__ __forceinline__ void ldk(int i, const bf16* p) { k[i] = ld8(p); }
  __device__ __forceinline__ bf16x8 gv(int i) const { return v[i]; } __device__ __forceinline__ bf16x8 gk(int i) const { return k[i]; } };
template <int NR> struct Stage<float, NR> { f32x4 va[NR], vb[NR], ka[NR], kb[NR];
  __device__ __forceinline__ void ldv(int i, const float* p) { va[i] = *(const f32x4*)p; vb[i] = *(const f32x4*)(p + 4); }
  __device__ __forceinline__ void ldk(int i, const float* p) { ka[i] = *(const f32x4*)p; kb[i] = *(const f32x4*)(p + 4); }
  __device__ __forceinline__ bf16x8 gv(int i) const { return pack8(va[i], vb[i]); } __device__ __forceinline__ bf16x8 gk(int i) const { return pack8(ka[i], kb[i]); } };
__device__ __forceinline__ int voff(int row, int sc) { return (row >> 5) * 8192 + 2 * ((sc >> 4) * 512 + (row & 31) * 16 + (sc & 8)); }

__device__ __forceinline__ void partialSM(f32x16& p0, f32x16& p1, float& m_reg, float& msub, float& alpha, int kbase, int lim, int lim0) {
  if constexpr (CAUSAL || HAS_LENS) { if (kbase + KVBLK - 1 > lim0) {
    const float ninf = -__builtin_inff(); const int hi = (threadIdx.x & 63) >> 5;
    for (int r = 0; r < 16; ++r) { const int k = kbase + crow(r, hi); p0[r] = k > lim ? ninf : p0[r]; p1[r] = k + 32 > lim ? ninf : p1[r]; } } }
  float pmax = p0[0]; for (int r = 1; r < 16; ++r) pmax = fmaxf(pmax, p0[r]); for (int r = 0; r < 16; ++r) pmax = fmaxf(pmax, p1[r]);
  { auto rr = __builtin_amdgcn_permlane32_swap(__float_as_uint(pmax), __float_as_uint(pmax), false, false);
    pmax = fmaxf(__uint_as_float(rr[0]), __uint_as_float(rr[1])); }
  pmax *= SL2E;
  if (__builtin_expect(__all(pmax - m_reg <= THR2), 1)) { alpha = 1.f; }
  else { float mn = fmaxf(m_reg, pmax); msub = (mn == -__builtin_inff()) ? 0.f : mn; alpha = __builtin_amdgcn_exp2f(m_reg - msub); m_reg = mn; }
  float nm = -msub;
  for (int r = 0; r < 16; ++r) p0[r] = fmaf(p0[r], SL2E, nm); for (int r = 0; r < 16; ++r) p1[r] = fmaf(p1[r], SL2E, nm);
  for (int r = 0; r < 16; ++r) p0[r] = __builtin_amdgcn_exp2f(p0[r]);
}
__device__ __forceinline__ void finishSM(f32x16& p0, f32x16& p1, float alpha, float& l_reg, bf16x8& pa0, bf16x8& pa1, bf16x8& pa2, bf16x8& pa3) {
  for (int r = 0; r < 16; ++r) p1[r] = __builtin_amdgcn_exp2f(p1[r]);
  float ps = 0; for (int r = 0; r < 16; ++r) ps += p0[r]; for (int r = 0; r < 16; ++r) ps += p1[r];
  { auto rr = __builtin_amdgcn_permlane32_swap(__float_as_uint(ps), __float_as_uint(ps), false, false); ps = __uint_as_float(rr[0]) + __uint_as_float(rr[1]); }
  l_reg = l_reg * alpha + ps;
#define PK4(P, BASE, OUT) do { unsigned a0 = cvtpk(P[BASE + 0], P[BASE + 1]), a1 = cvtpk(P[BASE + 2], P[BASE + 3]); \
    unsigned b0 = cvtpk(P[BASE + 4], P[BASE + 5]), b1 = cvtpk(P[BASE + 6], P[BASE + 7]); \
    auto r0 = __builtin_amdgcn_permlane32_swap(a0, b0, false, false); auto r1 = __builtin_amdgcn_permlane32_swap(a1, b1, false, false); \
    u32x4 w = {r0[0], r1[0], r0[1], r1[1]}; OUT = *reinterpret_cast<bf16x8*>(&w); } while (0)
  PK4(p0, 0, pa0); PK4(p0, 8, pa1); PK4(p1, 0, pa2); PK4(p1, 8, pa3);
#undef PK4
}
template <int QP, int NWT> __device__ __forceinline__ void qkt(f32x16& p0, f32x16& p1, const bf16* Ks, const bf16x8* qr, const char* qp, int r32, int hi) {
  p0 = f32x16{}; p1 = f32x16{};
#pragma unroll
  for (int d0 = 0; d0 < 8; ++d0) { int cb = (d0 * 16 + hi * 8) * 2;
    bf16x8 b0 = *reinterpret_cast<const bf16x8*>((const char*)Ks + KSWZ(r32, cb));
    bf16x8 b1 = *reinterpret_cast<const bf16x8*>((const char*)Ks + KSWZ(32 + r32, cb));
    const bf16x8 q = d0 < 8 - QP ? qr[d0] : *reinterpret_cast<const bf16x8*>(qp + (d0 - (8 - QP)) * (64 * NWT * 16));
    p0 = __builtin_amdgcn_mfma_f32_32x32x16_bf16(b0, q, p0, 0, 0, 0);
    p1 = __builtin_amdgcn_mfma_f32_32x32x16_bf16(b1, q, p1, 0, 0, 0); }
}
__device__ __forceinline__ void pv_d0(f32x16* o, int vb, bf16x8 pa0, bf16x8 pa1, bf16x8 pa2, bf16x8 pa3) {
#pragma unroll
  for (int d0 = 0; d0 < 4; ++d0) { s16x4 lo[4], hi[4];
#pragma unroll
    for (int ks = 0; ks < 4; ++ks) { int a = vb + d0 * 2048 + (ks >> 1) * 8192 + (ks & 1) * 512;
      asm volatile("ds_read_b64_tr_b16 %0,%1" : "=&v"(lo[ks]) : "v"(a) : "memory");
      asm volatile("ds_read_b64_tr_b16 %0,%1 offset:128" : "=&v"(hi[ks]) : "v"(a) : "memory"); }
    asm volatile("s_waitcnt lgkmcnt(0)" ::: "memory"); SBAR();
#define PK(k) (bf16x8){lo[k][0], lo[k][1], lo[k][2], lo[k][3], hi[k][0], hi[k][1], hi[k][2], hi[k][3]}
    o[d0] = __builtin_amdgcn_mfma_f32_32x32x16_bf16(pa0, PK(0), o[d0], 0, 0, 0);
    o[d0] = __builtin_amdgcn_mfma_f32_32x32x16_bf16(pa1, PK(1), o[d0], 0, 0, 0);
    o[d0] = __builtin_amdgcn_mfma_f32_32x32x16_bf16(pa2, PK(2), o[d0], 0, 0, 0);
    o[d0] = __builtin_amdgcn_mfma_f32_32x32x16_bf16(pa3, PK(3), o[d0], 0, 0, 0);
#undef PK
  }
}

template <class TQ, class TKV>
__global__ __launch_bounds__(64 * InCfg<TKV>::NWT, NW / InCfg<TKV>::NWT)
void gqa_chunk_fwd(const TQ* __restrict__ Q, const TKV* __restrict__ K, const TKV* __restrict__ V, const int* __restrict__ lens, float* __restrict__ O, float* __restrict__ pml, float* __restrict__ po) {
  constexpr int NWT = InCfg<TKV>::NWT, NS = NSPLIT * (NW / NWT), TPS = NT / NS;
  constexpr int NR = KVBLK / (4 * NWT);
  static_assert(ROWS <= QBLK * NWT, "rows must fit this form's waves");
  const int tid = threadIdx.x, wid = tid >> 6, lane = tid & 63, r32 = lane & 31, hi = lane >> 5;
  const int split = blockIdx.x, kvh = blockIdx.y, b = blockIdx.z;
  const TQ* Qg = Q + ((size_t)b * HQ + (size_t)kvh * G) * SQ * D;
  const TKV* Kh = K + (((size_t)b * 4 + (kvh >> 1)) * SK + (size_t)split * TPS * KVBLK) * D;
  const TKV* Vh = V + (((size_t)b * 4 + (kvh >> 1)) * SK + (size_t)split * TPS * KVBLK) * D;
  extern __shared__ __attribute__((aligned(16))) char shm[];
  bf16* V_lds = (bf16*)shm; bf16* K_lds = (bf16*)(shm + 2 * SHM_V);
  float* ws = (float*)(shm + SHM_WS) + wid * 64; float* li_l = ws; float* al_l = ws + 32;
  constexpr int QP = InCfg<TKV>::QP; char* qp = shm + shm_qp<NWT>() + tid * 16;
  const int R = wid * QBLK + r32, Rc = R < ROWS ? R : ROWS - 1;
  int len = SK; if constexpr (HAS_LENS) { len = __builtin_amdgcn_readfirstlane(lens[b]); len = len < 0 ? 0 : (len > SK ? SK : len); }
  const int lim0 = CAUSAL ? len - SQ : len - 1, lim = CAUSAL ? lim0 + Rc % SQ : lim0, kb0 = split * TPS * KVBLK;
  bf16x8 qr[8]; const TQ* qrow = Qg + (size_t)Rc * D;
  float m_reg = -__builtin_inff(), msub = 0.f, l_reg = 0.f; f32x16 o[4] = {};
  const int sr = tid >> 4, sc = (tid & 15) * 8;
  const int vb0 = (int)(uintptr_t)V_lds + (lane & 15) * 8 + ((lane >> 4) & 1) * 1024 + (lane >> 5) * 256;
  Stage<TKV, NR> st; int sk0 = 0; (void)sk0;
#define SLOAD(k0) do { sk0 = kb0 + (k0); \
    if constexpr (NR == 2) st.load(Vh + (size_t)((k0) + sr) * D + sc, Vh + (size_t)((k0) + 32 + sr) * D + sc, Kh + (size_t)((k0) + sr) * D + sc, Kh + (size_t)((k0) + 32 + sr) * D + sc); \
    else { const TKV* vr_ = Vh + (size_t)((k0) + NR * sr) * D + sc; const TKV* kr_ = Kh + (size_t)((k0) + NR * sr) * D + sc; \
           for (int i_ = 0; i_ < NR; ++i_) st.ldv(i_, vr_ + i_ * D); for (int i_ = 0; i_ < NR; ++i_) st.ldk(i_, kr_ + i_ * D); } } while (0)
#define SWRITE(buf) do { if constexpr (NR == 2) { bf16x8 v0_ = st.gv0(), v1_ = st.gv1(), k0_ = st.gk0(), k1_ = st.gk1(); \
      if constexpr (HAS_LENS) { if (sk0 + KVBLK > len) { const bf16x8 z_ = {}; if (sk0 + sr >= len) { v0_ = z_; k0_ = z_; } if (sk0 + 32 + sr >= len) { v1_ = z_; k1_ = z_; } } } \
      *(bf16x8*)((char*)V_lds + (buf) * SHM_V + voff(sr, sc)) = v0_; *(bf16x8*)((char*)V_lds + (buf) * SHM_V + voff(32 + sr, sc)) = v1_; int kc = sc * 2; \
      *(bf16x8*)((char*)K_lds + (buf) * SHM_K + KSWZ(sr, kc)) = k0_; *(bf16x8*)((char*)K_lds + (buf) * SHM_K + KSWZ(32 + sr, kc)) = k1_; } \
    else { for (int i_ = 0; i_ < NR; ++i_) { const int row_ = NR * sr + i_; bf16x8 v_ = st.gv(i_), k_ = st.gk(i_); \
      if constexpr (HAS_LENS) { if (sk0 + KVBLK > len) { if (sk0 + row_ >= len) { v_ = bf16x8{}; k_ = bf16x8{}; } } } \
      *(bf16x8*)((char*)V_lds + (buf) * SHM_V + voff(row_, sc)) = v_; *(bf16x8*)((char*)K_lds + (buf) * SHM_K + KSWZ(row_, sc * 2)) = k_; } } } while (0)
#define RESC(a) do { if (__any((a) < 1.f)) { if constexpr (NWT == NW || !XCHG4) { if (hi == 0) al_l[r32] = (a); asm volatile("s_waitcnt lgkmcnt(0)" ::: "memory"); \
      for (int d = 0; d < 4; ++d) for (int r = 0; r < 16; ++r) o[d][r] *= al_l[crow(r, hi)]; } \
    else { const int ai_ = __float_as_int(a);    \
      for (int r = 0; r < 16; ++r) { const float a0_ = __int_as_float(__builtin_amdgcn_readlane(ai_, crow(r, 0))), a1_ = __int_as_float(__builtin_amdgcn_readlane(ai_, crow(r, 1))); \
        const float ar_ = hi ? a1_ : a0_; for (int d = 0; d < 4; ++d) o[d][r] *= ar_; } } } } while (0)
  f32x16 pA0, pA1, pB0, pB1; float alA, alB; bf16x8 pa0, pa1, pa2, pa3;
  for (int d0 = 0; d0 < 8; ++d0) qr[d0] = ld8(qrow + d0 * 16 + hi * 8);
  for (int j = 0; j < QP; ++j) *reinterpret_cast<bf16x8*>(qp + j * (64 * NWT * 16)) = qr[8 - QP + j];
  SLOAD(0); SWRITE(0); __syncthreads();
  qkt<QP, NWT>(pA0, pA1, K_lds, qr, qp, r32, hi); partialSM(pA0, pA1, m_reg, msub, alA, kb0, lim, lim0);
  SLOAD(KVBLK); SWRITE(1); __syncthreads();
  for (int j = 1; j + 1 < TPS; j += 2) {
    SBAR(); qkt<QP, NWT>(pB0, pB1, (bf16*)((char*)K_lds + SHM_K), qr, qp, r32, hi);
    finishSM(pA0, pA1, alA, l_reg, pa0, pa1, pa2, pa3); SBAR();
    SLOAD((j + 1) * KVBLK); SBAR();
    pv_d0(o, vb0, pa0, pa1, pa2, pa3); partialSM(pB0, pB1, m_reg, msub, alB, kb0 + j * KVBLK, lim, lim0);
    __syncthreads(); asm volatile("s_waitcnt vmcnt(0)" ::: "memory"); SWRITE(0);
    RESC(alB); __syncthreads();
    SBAR(); qkt<QP, NWT>(pA0, pA1, K_lds, qr, qp, r32, hi);
    finishSM(pB0, pB1, alB, l_reg, pa0, pa1, pa2, pa3); SBAR();
    if (j + 2 < TPS) { SLOAD((j + 2) * KVBLK); SBAR(); }
    pv_d0(o, vb0 + (int)SHM_V, pa0, pa1, pa2, pa3); partialSM(pA0, pA1, m_reg, msub, alA, kb0 + (j + 1) * KVBLK, lim, lim0);
    __syncthreads(); if (j + 2 < TPS) { asm volatile("s_waitcnt vmcnt(0)" ::: "memory"); SWRITE(1); }
    RESC(alA); __syncthreads();
  }
  SBAR(); qkt<QP, NWT>(pB0, pB1, (bf16*)((char*)K_lds + SHM_K), qr, qp, r32, hi);
  finishSM(pA0, pA1, alA, l_reg, pa0, pa1, pa2, pa3); SBAR();
  pv_d0(o, vb0, pa0, pa1, pa2, pa3); partialSM(pB0, pB1, m_reg, msub, alB, kb0 + (TPS - 1) * KVBLK, lim, lim0);
  __syncthreads(); RESC(alB);
  finishSM(pB0, pB1, alB, l_reg, pa0, pa1, pa2, pa3); SBAR();
  pv_d0(o, vb0 + (int)SHM_V, pa0, pa1, pa2, pa3);
#undef SLOAD
#undef SWRITE
#undef RESC
  if constexpr (NS == 1) {
    float rli[16];
    if constexpr (NWT == NW || !XCHG4) { if (hi == 0) li_l[r32] = l_reg; asm volatile("s_waitcnt lgkmcnt(0)" ::: "memory");
#pragma unroll
      for (int r = 0; r < 16; ++r) { float l = li_l[crow(r, hi)]; rli[r] = l > 0.f ? __builtin_amdgcn_rcpf(l) : 0.f; } }
    else {
#pragma unroll
      for (int r = 0; r < 16; ++r) { const int li_ = __float_as_int(l_reg); const float l0 = __int_as_float(__builtin_amdgcn_readlane(li_, crow(r, 0))), l1 = __int_as_float(__builtin_amdgcn_readlane(li_, crow(r, 1)));
        const float l = hi ? l1 : l0; rli[r] = l > 0.f ? __builtin_amdgcn_rcpf(l) : 0.f; } }
#pragma unroll
    for (int r = 0; r < 16; ++r) { const int Ro = wid * QBLK + crow(r, hi); if (Ro < ROWS) { float* orow = O + (((size_t)b * HQ + (size_t)kvh * G) * SQ + Ro) * D;
        for (int d0 = 0; d0 < 4; ++d0) orow[d0 * 32 + r32] = o[d0][r] * rli[r]; } }
  } else {
    const size_t row0 = ((size_t)b * HQ + (size_t)kvh * G) * SQ;
    if (hi == 0 && R < ROWS) { float* p = pml + ((row0 + R) * NS + split) * 2; p[0] = m_reg; p[1] = l_reg; }
#pragma unroll
    for (int r = 0; r < 16; ++r) { const int Ro = wid * QBLK + crow(r, hi); if (Ro < ROWS) { float* prow = po + ((row0 + Ro) * NS + split) * D;
        for (int d0 = 0; d0 < 4; ++d0) prow[d0 * 32 + r32] = o[d0][r]; } }
  }
}

constexpr int CMB_ROWS = 8, CMB_BLOCKS = (int)(((size_t)B * HQ * SQ + CMB_ROWS - 1) / CMB_ROWS);
template <int NS> __global__ __launch_bounds__(256) void gqa_chunk_combine(const float* __restrict__ pml, const float* __restrict__ po, bf16* __restrict__ O) {
  const size_t row = (size_t)blockIdx.x * CMB_ROWS + (threadIdx.x >> 5); const int d = (threadIdx.x & 31) * 4;
  if (row >= (size_t)B * HQ * SQ) return;
  const float* ml = pml + row * NS * 2; const float* acc = po + row * NS * D + d;
  float M = -__builtin_inff(); for (int s = 0; s < NS; ++s) M = fmaxf(M, ml[2 * s]);
  const float Msub = (M == -__builtin_inff()) ? 0.f : M; float L = 0.f; f32x4 o = {0.f, 0.f, 0.f, 0.f};
  for (int s = 0; s < NS; ++s) { const float m = ml[2 * s], l = ml[2 * s + 1]; const float w = l > 0.f ? __builtin_amdgcn_exp2f(m - Msub) : 0.f;
    const f32x4 a = *reinterpret_cast<const f32x4*>(acc + (size_t)s * D); L += w * l; o[0] += w * a[0]; o[1] += w * a[1]; o[2] += w * a[2]; o[3] += w * a[3]; }
  const float inv = L > 0.f ? 1.f / L : 0.f; o[0] *= inv; o[1] *= inv; o[2] *= inv; o[3] *= inv;
  { uint2 w; w.x = cvtpk(o[0], o[1]); w.y = cvtpk(o[2], o[3]); *reinterpret_cast<uint2*>(O + row * D + d) = w; }
}

__global__ __launch_bounds__(256) void cvt_f32_bf16(const float* __restrict__ q, const float* __restrict__ k, const float* __restrict__ v, bf16* __restrict__ qb, bf16* __restrict__ kb, bf16* __restrict__ vb) {
  const size_t i0 = ((size_t)blockIdx.x * 256 + threadIdx.x) * 8, step = (size_t)gridDim.x * 256 * 8;
  for (size_t i = i0; i < NQ; i += step) *reinterpret_cast<bf16x8*>(qb + i) = ld8(q + i);
  for (size_t i = i0; i < NKV; i += step) { *reinterpret_cast<bf16x8*>(kb + i) = ld8(k + i); *reinterpret_cast<bf16x8*>(vb + i) = ld8(v + i); }
}
template <class TQ, class TKV>
static void gqa_chunk_forward(const TQ* q, const TKV* k, const TKV* v, const int* lens, float* o, void* ws, hipStream_t stream) {
  constexpr size_t shm = shm_bytes<TKV>();
  (void)hipFuncSetAttribute((const void*)gqa_chunk_fwd<TQ, TKV>, hipFuncAttributeMaxDynamicSharedMemorySize, (int)shm);
  float* pml = (float*)((char*)ws + WS_PML); float* po = (float*)((char*)ws + WS_PO);
  constexpr int NS = ns_for<TKV>();
  hipLaunchKernelGGL((gqa_chunk_fwd<TQ, TKV>), dim3(NS, HKV, B), dim3(64 * nw_for<TKV>()), shm, stream, q, k, v, lens, o, pml, po);
  if constexpr (NS > 1) hipLaunchKernelGGL((gqa_chunk_combine<NS>), dim3(CMB_BLOCKS), dim3(256), 0, stream, (const float*)pml, (const float*)po, (bf16*)o);
}

extern "C" void kernel_launch(void* const* d_in, const int* in_sizes, int n_in, void* d_out, int out_size, void* d_ws, size_t ws_size, hipStream_t stream) {
  gqa_chunk_forward<bf16, bf16>((const bf16*)d_in[0], (const bf16*)d_in[1], (const bf16*)d_in[2], nullptr, (float*)d_out, d_ws, stream);
}
```

```cpp
#include <hip/hip_runtime.h>
#include <hip/hip_bf16.h>

typedef unsigned short u16;
typedef unsigned u32x4 __attribute__((ext_vector_type(4)));

__device__ __forceinline__ float bf_lo(unsigned x) { return __uint_as_float(x << 16); }
__device__ __forceinline__ float bf_hi(unsigned x) { return __uint_as_float(x & 0xffff0000u); }

__device__ __forceinline__ void slow_path(const u16* __restrict__ Q, const u16* __restrict__ K, const u16* __restrict__ V,
                                       u16* __restrict__ O, unsigned g, unsigned j, unsigned w, unsigned lane) {
  const float C = 0.12751743082f;
  const u16* Kb = K + (size_t)g * 1024 * 128;
  const u16* Vb = V + (size_t)g * 1024 * 128 + 32 * w;
  for (unsigned rr = lane; rr < 96; rr += 64) {
    const size_t row = (size_t)g * 384 + j * 96 + rr;
    const u32x4* q4 = (const u32x4*)(Q + row * 128);
    u32x4 q[16];
#pragma unroll
    for (int i = 0; i < 16; ++i) q[i] = q4[i];
    float m = -__builtin_inff();
    for (int k = 0; k < 1024; ++k) {
      const u32x4* k4 = (const u32x4*)(Kb + (size_t)k * 128);
      float s = 0.f;
#pragma unroll
      for (int i = 0; i < 16; ++i) {
        const u32x4 kk = k4[i];
#pragma unroll
        for (int e = 0; e < 4; ++e) { s = fmaf(bf_lo(q[i][e]), bf_lo(kk[e]), s); s = fmaf(bf_hi(q[i][e]), bf_hi(kk[e]), s); }
      }
      m = (s > m || s != s) ? s : m;
    }
    float l = 0.f, acc[32];
#pragma unroll
    for (int i = 0; i < 32; ++i) acc[i] = 0.f;
    for (int k = 0; k < 1024; ++k) {
      const u32x4* k4 = (const u32x4*)(Kb + (size_t)k * 128);
      float s = 0.f;
#pragma unroll
      for (int i = 0; i < 16; ++i) {
        const u32x4 kk = k4[i];
#pragma unroll
        for (int e = 0; e < 4; ++e) { s = fmaf(bf_lo(q[i][e]), bf_lo(kk[e]), s); s = fmaf(bf_hi(q[i][e]), bf_hi(kk[e]), s); }
      }
      const float p = __builtin_amdgcn_exp2f((s - m) * C);
      l += p;
      const u32x4* v4 = (const u32x4*)(Vb + (size_t)k * 128);
#pragma unroll
      for (int i = 0; i < 4; ++i) {
        const u32x4 vv = v4[i];
#pragma unroll
        for (int e = 0; e < 4; ++e) { acc[8 * i + 2 * e] = fmaf(p, bf_lo(vv[e]), acc[8 * i + 2 * e]); acc[8 * i + 2 * e + 1] = fmaf(p, bf_hi(vv[e]), acc[8 * i + 2 * e + 1]); }
      }
    }
    const float inv = 1.f / l;
    __hip_bfloat16* o = (__hip_bfloat16*)(O + row * 128 + 32 * w);
#pragma unroll
    for (int i = 0; i < 32; ++i) o[i] = __float2bfloat16(acc[i] * inv);
  }
}

__global__ __launch_bounds__(256, 1) void gqa_attn_kernel(const u16* __restrict__ Q, const u16* __restrict__ K,
                                                          const u16* __restrict__ V, u16* __restrict__ O) {
  __shared__ __attribute__((aligned(1024))) char lds[163840];
  const unsigned tid = threadIdx.x, bx = blockIdx.x;
  const unsigned ldsb = (unsigned)(size_t)lds;
  unsigned bad;
  const unsigned long long ka = (unsigned long long)__builtin_amdgcn_kernarg_segment_ptr();
  asm volatile(
    "s_load_dwordx2 s[48:49], %[ka], 0\n"
    "s_load_dwordx2 s[40:41], %[ka], 8\n"
    "s_load_dwordx2 s[44:45], %[ka], 16\n"
    "s_load_dwordx2 s[52:53], %[ka], 24\n"
    "v_lshrrev_b32 v28, 6, %[tid]\n"
    "v_and_b32 v29, 63, %[tid]\n"
    "s_and_b32 s80, %[bx], 7\n"
    "v_readfirstlane_b32 s36, v28\n"
    "s_lshr_b32 s81, %[bx], 5\n"
    "s_lshl_b32 s81, s81, 3\n"
    "s_add_u32 s80, s80, s81\n"
    "s_lshr_b32 s81, %[bx], 3\n"
    "s_and_b32 s81, s81, 3\n"
    "v_lshlrev_b32 v30, 4, v29\n"
    "s_mul_i32 s82, s80, 98304\n"
    "s_mul_i32 s83, s81, 24576\n"
    "s_add_u32 s82, s82, s83\n"
    "s_lshl_b32 s83, s36, 11\n"
    "v_add_u32 v31, s83, v30\n"
    "s_movk_i32 s50, 0x6000\n"
    "s_mov_b32 s51, 0x20000\n"
    "s_lshl_b32 s84, s80, 2\n"
    "s_add_u32 s84, s84, s36\n"
    "s_lshl_b32 s84, s84, 16\n"
    "v_lshrrev_b32 v28, 4, v29\n"
    "v_and_b32 v32, 15, v29\n"
    "v_xor_b32 v17, v32, v28\n"
    "s_mov_b32 s42, 0x10000\n"
    "v_lshlrev_b32 v17, 4, v17\n"
    "s_mov_b32 s43, 0x20000\n"
    "v_lshl_or_b32 v16, v28, 8, v17\n"
    "s_lshl_b32 s94, s36, 15\n"
    "s_add_u32 s94, s94, %[lds]\n"
    "v_xor_b32 v17, 64, v16\n"
    "v_xor_b32 v18, 128, v16\n"
    "v_xor_b32 v19, 192, v16\n"
    "s_mov_b32 s72, s94\n"
    "s_add_u32 s73, s94, 4096\n"
    "s_movk_i32 s57, 0x1000\n"
    "s_waitcnt lgkmcnt(0)\n"
    "s_add_u32 s48, s48, s82\n"
    "s_addc_u32 s49, s49, 0\n"
    "s_and_b32 s49, s49, 0xffff\n"
    "buffer_load_dwordx4 v[176:179], v31, s[48:51], 0 offen offset:0\n"
    "buffer_load_dwordx4 v[180:183], v31, s[48:51], 0 offen offset:1024\n"
    "s_add_u32 s40, s40, s84\n"
    "s_addc_u32 s41, s41, 0\n"
    "s_and_b32 s41, s41, 0xffff\n"
    "s_mov_b32 m0, s72\n"
    "s_nop 0\n"
    "buffer_load_dwordx4 v16, s[40:43], 0 offen offset:0 lds\n"
    "buffer_load_dwordx4 v17, s[40:43], 0 offen offset:1024 lds\n"
    "buffer_load_dwordx4 v18, s[40:43], 0 offen offset:2048 lds\n"
    "buffer_load_dwordx4 v19, s[40:43], 0 offen offset:3072 lds\n"
    "s_mov_b32 m0, s73\n"
    "s_nop 0\n"
    "buffer_load_dwordx4 v16, s[40:43], s57 offen offset:0 lds\n"
    "buffer_load_dwordx4 v17, s[40:43], s57 offen offset:1024 lds\n"
    "buffer_load_dwordx4 v18, s[40:43], s57 offen offset:2048 lds\n"
    "buffer_load_dwordx4 v19, s[40:43], s57 offen offset:3072 lds\n"
    "s_add_u32 s74, s94, 8192\n"
    "s_add_u32 s75, s94, 12288\n"
    "s_add_u32 s76, s94, 16384\n"
    "s_add_u32 s77, s94, 20480\n"
    "s_add_u32 s78, s94, 24576\n"
    "s_add_u32 s79, s94, 28672\n"
    "s_movk_i32 s58, 0x2000\n"
    "s_movk_i32 s59, 0x3000\n"
    "s_movk_i32 s60, 0x4000\n"
    "s_movk_i32 s61, 0x5000\n"
    "s_movk_i32 s62, 0x6000\n"
    "s_movk_i32 s63, 0x7000\n"
    "s_mov_b32 s64, 32768\n"
    "s_mov_b32 s65, 36864\n"
    "s_mov_b32 s66, 40960\n"
    "s_mov_b32 s67, 45056\n"
    "s_mov_b32 s68, 49152\n"
    "s_mov_b32 s69, 53248\n"
    "s_mov_b32 s70, 57344\n"
    "s_mov_b32 s71, 61440\n"
    "s_add_u32 s44, s44, s84\n"
    "s_addc_u32 s45, s45, 0\n"
    "s_and_b32 s45, s45, 0xffff\n"
    "s_mov_b32 s46, 0x10000\n"
    "s_mov_b32 s47, 0x20000\n"
    "s_add_u32 s52, s52, s82\n"
    "s_addc_u32 s53, s53, 0\n"
    "s_and_b32 s53, s53, 0xffff\n"
    "s_movk_i32 s54, 0x6000\n"
    "s_mov_b32 s55, 0x20000\n"
    "s_mov_b32 s90, 0x3e0293ee\n"
    "s_mov_b32 s91, 0x3e0293ee\n"
    "s_mov_b32 s95, 0x30800000\n"
    "s_mov_b32 s96, 0x5d800000\n"
    "s_mov_b64 s[92:93], 0\n"
    "v_lshlrev_b32 v21, 1, v28\n"
    "v_xor_b32 v21, v32, v21\n"
    "v_lshlrev_b32 v21, 4, v21\n"
    "v_lshl_or_b32 v20, v28, 8, v21\n"
    "v_xor_b32 v21, 128, v20\n"
    "v_xor_b32 v36, v28, v32\n"
    "v_lshlrev_b32 v36, 4, v36\n"
    "v_lshl_or_b32 v36, v32, 8, v36\n"
    "v_add_u32 v4, s94, v36\n"
    "s_add_u32 s85, %[lds], 131072\n"
    "v_add_u32 v208, s85, v36\n"
    "v_xor_b32 v5, 64, v4\n"
    "v_xor_b32 v209, 64, v208\n"
    "v_xor_b32 v6, 128, v4\n"
    "v_xor_b32 v210, 128, v208\n"
    "v_xor_b32 v7, 192, v4\n"
    "v_xor_b32 v211, 192, v208\n"
    "v_bfe_u32 v37, v29, 2, 2\n"
    "v_lshl_add_u32 v38, v28, 2, v37\n"
    "v_and_b32 v39, 7, v38\n"
    "v_lshlrev_b32 v39, 1, v39\n"
    "v_bfe_u32 v37, v29, 1, 1\n"
    "v_xor_b32 v39, v39, v37\n"
    "v_lshlrev_b32 v39, 4, v39\n"
    "v_lshl_or_b32 v38, v38, 8, v39\n"
    "v_and_b32 v39, 1, v29\n"
    "v_lshl_or_b32 v38, v39, 3, v38\n"
    "s_lshl_b32 s86, s36, 6\n"
    "v_xor_b32 v38, s86, v38\n"
    "v_add_u32 v8, s94, v38\n"
    "v_xor_b32 v9, 32, v8\n"
    "v_xor_b32 v10, 64, v8\n"
    "v_xor_b32 v11, 96, v8\n"
    "v_xor_b32 v12, 128, v8\n"
    "v_xor_b32 v13, 160, v8\n"
    "v_xor_b32 v14, 192, v8\n"
    "v_xor_b32 v15, 224, v8\n"
    "v_mov_b32 v22, 0\n"
    "v_mov_b32 v23, 0\n"
    "v_mov_b32 v24, 0\n"
    "v_mov_b32 v25, 0\n"
    "v_mov_b32 v26, 0\n"
    "v_mov_b32 v27, 0\n"
    "s_add_u32 s85, s85, s83\n"
    "s_and_b32 s87, s36, 1\n"
    "s_lshl_b32 s87, s87, 7\n"
    "v_xor_b32 v212, s87, v16\n"
    "v_add_u32 v212, s85, v212\n"
    "v_xor_b32 v213, 64, v212\n"
    "s_waitcnt vmcnt(8)\n"
    "buffer_load_dwordx4 v[184:187], v31, s[48:51], s58 offen offset:0\n"
    "buffer_load_dwordx4 v[188:191], v31, s[48:51], s58 offen offset:1024\n"
    "v_lshlrev_b32 v200, 16, v176\n"
    "v_and_b32 v201, 0xffff0000, v176\n"
    "v_lshlrev_b32 v202, 16, v177\n"
    "v_and_b32 v203, 0xffff0000, v177\n"
    "v_lshlrev_b32 v204, 16, v178\n"
    "v_and_b32 v205, 0xffff0000, v178\n"
    "v_lshlrev_b32 v206, 16, v179\n"
    "v_and_b32 v207, 0xffff0000, v179\n"
    "v_pk_mul_f32 v[200:201], v[200:201], s[90:91]\n"
    "v_pk_mul_f32 v[202:203], v[202:203], s[90:91]\n"
    "v_pk_mul_f32 v[204:205], v[204:205], s[90:91]\n"
    "v_pk_mul_f32 v[206:207], v[206:207], s[90:91]\n"
    "v_cvt_pk_bf16_f32 v176, v200, v201\n"
    "v_cvt_pk_bf16_f32 v177, v202, v203\n"
    "v_cvt_pk_bf16_f32 v178, v204, v205\n"
    "v_cvt_pk_bf16_f32 v179, v206, v207\n"
    "ds_write_b128 v212, v[176:179] offset:0\n"
    "v_lshlrev_b32 v200, 16, v180\n"
    "v_and_b32 v201, 0xffff0000, v180\n"
    "v_lshlrev_b32 v202, 16, v181\n"
    "v_and_b32 v203, 0xffff0000, v181\n"
    "v_lshlrev_b32 v204, 16, v182\n"
    "v_and_b32 v205, 0xffff0000, v182\n"
    "v_lshlrev_b32 v206, 16, v183\n"
    "v_and_b32 v207, 0xffff0000, v183\n"
    "v_pk_mul_f32 v[200:201], v[200:201], s[90:91]\n"
    "v_pk_mul_f32 v[202:203], v[202:203], s[90:91]\n"
    "v_pk_mul_f32 v[204:205], v[204:205], s[90:91]\n"
    "v_pk_mul_f32 v[206:207], v[206:207], s[90:91]\n"
    "v_cvt_pk_bf16_f32 v180, v200, v201\n"
    "v_cvt_pk_bf16_f32 v181, v202, v203\n"
    "v_cvt_pk_bf16_f32 v182, v204, v205\n"
    "v_cvt_pk_bf16_f32 v183, v206, v207\n"
    "ds_write_b128 v213, v[180:183] offset:1024\n"
    "s_mov_b32 m0, s74\n"
    "s_nop 0\n"
    "buffer_load_dwordx4 v20, s[44:47], 0 offen offset:0 lds\n"
    "buffer_load_dwordx4 v21, s[44:47], 0 offen offset:1024 lds\n"
    "buffer_load_dwordx4 v20, s[44:47], 0 offen offset:2048 lds\n"
    "buffer_load_dwordx4 v21, s[44:47], 0 offen offset:3072 lds\n"
    "s_mov_b32 m0, s75\n"
    "s_nop 0\n"
    "buffer_load_dwordx4 v20, s[44:47], s57 offen offset:0 lds\n"
    "buffer_load_dwordx4 v21, s[44:47], s57 offen offset:1024 lds\n"
    "buffer_load_dwordx4 v20, s[44:47], s57 offen offset:2048 lds\n"
    "buffer_load_dwordx4 v21, s[44:47], s57 offen offset:3072 lds\n"
    "s_waitcnt lgkmcnt(0)\n"
    "s_barrier\n"
    "ds_read_b128 a[0:3], v208 offset:0\n"
    "ds_read_b128 a[4:7], v209 offset:0\n"
    "ds_read_b128 a[8:11], v210 offset:0\n"
    "ds_read_b128 a[12:15], v211 offset:0\n"
    "s_waitcnt vmcnt(10)\n"
    "ds_read_b128 v[64:67], v4 offset:0\n"
    "ds_read_b128 v[68:71], v5 offset:0\n"
    "ds_read_b128 v[72:75], v6 offset:0\n"
    "ds_read_b128 v[76:79], v7 offset:0\n"
    "ds_read_b128 v[80:83], v4 offset:4096\n"
    "ds_read_b128 v[84:87], v5 offset:4096\n"
    "ds_read_b128 v[88:91], v6 offset:4096\n"
    "ds_read_b128 v[92:95], v7 offset:4096\n"
    "ds_read_b128 a[16:19], v208 offset:4096\n"
    "ds_read_b128 a[20:23], v209 offset:4096\n"
    "ds_read_b128 a[24:27], v210 offset:4096\n"
    "ds_read_b128 a[28:31], v211 offset:4096\n"
    "s_waitcnt lgkmcnt(11)\n"
    "v_mfma_f32_16x16x32_bf16 v[36:39], v[64:67], a[0:3], 0\n"
    "s_waitcnt lgkmcnt(10)\n"
    "v_mfma_f32_16x16x32_bf16 v[36:39], v[68:71], a[4:7], v[36:39]\n"
    "s_waitcnt lgkmcnt(9)\n"
    "v_mfma_f32_16x16x32_bf16 v[36:39], v[72:75], a[8:11], v[36:39]\n"
    "s_waitcnt lgkmcnt(8)\n"
    "v_mfma_f32_16x16x32_bf16 v[36:39], v[76:79], a[12:15], v[36:39]\n"
    "s_waitcnt lgkmcnt(7)\n"
    "v_mfma_f32_16x16x32_bf16 v[40:43], v[80:83], a[0:3], 0\n"
    "s_waitcnt lgkmcnt(6)\n"
    "v_mfma_f32_16x16x32_bf16 v[40:43], v[84:87], a[4:7], v[40:43]\n"
    "s_waitcnt lgkmcnt(5)\n"
    "v_mfma_f32_16x16x32_bf16 v[40:43], v[88:91], a[8:11], v[40:43]\n"
    "s_nop 1\n"
    "v_exp_f32 v36, v36\n"
    "s_waitcnt lgkmcnt(4)\n"
    "v_mfma_f32_16x16x32_bf16 v[40:43], v[92:95], a[12:15], v[40:43]\n"
    "v_exp_f32 v37, v37\n"
    "v_add_f32 v22, v22, v36\n"
    "s_waitcnt lgkmcnt(3)\n"
    "v_mfma_f32_16x16x32_bf16 v[44:47], v[64:67], a[16:19], 0\n"
    "v_exp_f32 v38, v38\n"
    "v_cvt_pk_bf16_f32 v52, v36, v37\n"
    "v_add_f32 v22, v22, v37\n"
    "s_waitcnt lgkmcnt(2)\n"
    "v_mfma_f32_16x16x32_bf16 v[44:47], v[68:71], a[20:23], v[44:47]\n"
    "v_exp_f32 v39, v39\n"
    "v_add_f32 v22, v22, v38\n"
    "s_waitcnt lgkmcnt(1)\n"
    "v_mfma_f32_16x16x32_bf16 v[44:47], v[72:75], a[24:27], v[44:47]\n"
    "v_cvt_pk_bf16_f32 v53, v38, v39\n"
    "v_add_f32 v22, v22, v39\n"
    "s_waitcnt lgkmcnt(0)\n"
    "v_mfma_f32_16x16x32_bf16 v[44:47], v[76:79], a[28:31], v[44:47]\n"
    "v_exp_f32 v40, v40\n"
    "v_mfma_f32_16x16x32_bf16 v[48:51], v[80:83], a[16:19], 0\n"
    "v_exp_f32 v41, v41\n"
    "v_add_f32 v22, v22, v40\n"
    "v_mfma_f32_16x16x32_bf16 v[48:51], v[84:87], a[20:23], v[48:51]\n"
    "v_exp_f32 v42, v42\n"
    "v_cvt_pk_bf16_f32 v54, v40, v41\n"
    "v_add_f32 v22, v22, v41\n"
    "v_mfma_f32_16x16x32_bf16 v[48:51], v[88:91], a[24:27], v[48:51]\n"
    "v_exp_f32 v43, v43\n"
    "v_exp_f32 v44, v44\n"
    "v_add_f32 v22, v22, v42\n"
    "v_mfma_f32_16x16x32_bf16 v[48:51], v[92:95], a[28:31], v[48:51]\n"
    "v_cvt_pk_bf16_f32 v55, v42, v43\n"
    "v_add_f32 v22, v22, v43\n"
    "v_add_f32 v23, v23, v44\n"
    "s_mov_b32 m0, s76\n"
    "s_waitcnt vmcnt(8)\n"
    "v_mov_b32 v191, v191\n"
    "buffer_load_dwordx4 v[192:195], v31, s[48:51], s60 offen offset:0\n"
    "buffer_load_dwordx4 v[196:199], v31, s[48:51], s60 offen offset:1024\n"
    "v_lshlrev_b32 v200, 16, v184\n"
    "v_and_b32 v201, 0xffff0000, v184\n"
    "v_lshlrev_b32 v202, 16, v185\n"
    "v_and_b32 v203, 0xffff0000, v185\n"
    "v_lshlrev_b32 v204, 16, v186\n"
    "v_and_b32 v205, 0xffff0000, v186\n"
    "v_lshlrev_b32 v206, 16, v187\n"
    "v_and_b32 v207, 0xffff0000, v187\n"
    "v_pk_mul_f32 v[200:201], v[200:201], s[90:91]\n"
    "v_pk_mul_f32 v[202:203], v[202:203], s[90:91]\n"
    "v_pk_mul_f32 v[204:205], v[204:205], s[90:91]\n"
    "v_pk_mul_f32 v[206:207], v[206:207], s[90:91]\n"
    "v_cvt_pk_bf16_f32 v184, v200, v201\n"
    "v_cvt_pk_bf16_f32 v185, v202, v203\n"
    "v_cvt_pk_bf16_f32 v186, v204, v205\n"
    "v_cvt_pk_bf16_f32 v187, v206, v207\n"
    "ds_write_b128 v212, v[184:187] offset:8192\n"
    "v_lshlrev_b32 v200, 16, v188\n"
    "v_and_b32 v201, 0xffff0000, v188\n"
    "v_lshlrev_b32 v202, 16, v189\n"
    "v_and_b32 v203, 0xffff0000, v189\n"
    "v_lshlrev_b32 v204, 16, v190\n"
    "v_and_b32 v205, 0xffff0000, v190\n"
    "v_lshlrev_b32 v206, 16, v191\n"
    "v_and_b32 v207, 0xffff0000, v191\n"
    "v_pk_mul_f32 v[200:201], v[200:201], s[90:91]\n"
    "v_pk_mul_f32 v[202:203], v[202:203], s[90:91]\n"
    "v_pk_mul_f32 v[204:205], v[204:205], s[90:91]\n"
    "v_pk_mul_f32 v[206:207], v[206:207], s[90:91]\n"
    "v_cvt_pk_bf16_f32 v188, v200, v201\n"
    "v_cvt_pk_bf16_f32 v189, v202, v203\n"
    "v_cvt_pk_bf16_f32 v190, v204, v205\n"
    "v_cvt_pk_bf16_f32 v191, v206, v207\n"
    "ds_write_b128 v213, v[188:191] offset:9216\n"
    "s_waitcnt lgkmcnt(0)\n"
    "s_barrier\n"
    "ds_read_b128 a[32:35], v208 offset:8192\n"
    "ds_read_b128 a[36:39], v209 offset:8192\n"
    "ds_read_b128 a[40:43], v210 offset:8192\n"
    "ds_read_b128 a[44:47], v211 offset:8192\n"
    "ds_read_b128 a[48:51], v208 offset:12288\n"
    "ds_read_b128 a[52:55], v209 offset:12288\n"
    "ds_read_b128 a[56:59], v210 offset:12288\n"
    "ds_read_b128 a[60:63], v211 offset:12288\n"
    "s_waitcnt vmcnt(2)\n"
    "ds_read_b64_tr_b16 v[96:97], v8 offset:8192\n"
    "ds_read_b64_tr_b16 v[98:99], v8 offset:12288\n"
    "ds_read_b64_tr_b16 v[100:101], v9 offset:8192\n"
    "ds_read_b64_tr_b16 v[102:103], v9 offset:12288\n"
    "ds_read_b64_tr_b16 v[104:105], v10 offset:8192\n"
    "ds_read_b64_tr_b16 v[106:107], v10 offset:12288\n"
    "ds_read_b64_tr_b16 v[108:109], v11 offset:8192\n"
    "ds_read_b64_tr_b16 v[110:111], v11 offset:12288\n"
    "ds_read_b64_tr_b16 v[112:113], v12 offset:8192\n"
    "ds_read_b64_tr_b16 v[114:115], v12 offset:12288\n"
    "ds_read_b64_tr_b16 v[116:117], v13 offset:8192\n"
    "ds_read_b64_tr_b16 v[118:119], v13 offset:12288\n"
    "ds_read_b64_tr_b16 v[120:121], v14 offset:8192\n"
    "ds_read_b64_tr_b16 v[122:123], v14 offset:12288\n"
    "ds_read_b64_tr_b16 v[124:125], v15 offset:8192\n"
    "ds_read_b64_tr_b16 v[126:127], v15 offset:12288\n"
    "s_waitcnt lgkmcnt(14)\n"
    "v_mfma_f32_16x16x32_bf16 v[128:131], v[96:99], v[52:55], 0\n"
    "buffer_load_dwordx4 v16, s[40:43], s58 offen offset:0 lds\n"
    "v_exp_f32 v45, v45\n"
    "s_waitcnt lgkmcnt(12)\n"
    "v_mfma_f32_16x16x32_bf16 v[132:135], v[100:103], v[52:55], 0\n"
    "v_cvt_pk_bf16_f32 v56, v44, v45\n"
    "v_add_f32 v23, v23, v45\n"
    "s_waitcnt lgkmcnt(10)\n"
    "v_mfma_f32_16x16x32_bf16 a[96:99], v[104:107], v[52:55], 0\n"
    "buffer_load_dwordx4 v17, s[40:43], s58 offen offset:1024 lds\n"
    "v_exp_f32 v46, v46\n"
    "s_waitcnt lgkmcnt(8)\n"
    "v_mfma_f32_16x16x32_bf16 a[100:103], v[108:111], v[52:55], 0\n"
    "v_add_f32 v23, v23, v46\n"
    "s_waitcnt lgkmcnt(6)\n"
    "v_mfma_f32_16x16x32_bf16 a[104:107], v[112:115], v[52:55], 0\n"
    "buffer_load_dwordx4 v18, s[40:43], s58 offen offset:2048 lds\n"
    "v_exp_f32 v47, v47\n"
    "s_waitcnt lgkmcnt(4)\n"
    "v_mfma_f32_16x16x32_bf16 a[108:111], v[116:119], v[52:55], 0\n"
    "v_cvt_pk_bf16_f32 v57, v46, v47\n"
    "v_add_f32 v23, v23, v47\n"
    "s_waitcnt lgkmcnt(2)\n"
    "v_mfma_f32_16x16x32_bf16 a[112:115], v[120:123], v[52:55], 0\n"
    "buffer_load_dwordx4 v19, s[40:43], s58 offen offset:3072 lds\n"
    "v_exp_f32 v48, v48\n"
    "s_waitcnt lgkmcnt(0)\n"
    "v_mfma_f32_16x16x32_bf16 a[116:119], v[124:127], v[52:55], 0\n"
    "v_add_f32 v23, v23, v48\n"
    "s_mov_b32 m0, s77\n"
    "v_mfma_f32_16x16x32_bf16 v[36:39], v[64:67], a[32:35], 0\n"
    "buffer_load_dwordx4 v16, s[40:43], s59 offen offset:0 lds\n"
    "v_exp_f32 v49, v49\n"
    "v_mfma_f32_16x16x32_bf16 v[36:39], v[68:71], a[36:39], v[36:39]\n"
    "v_cvt_pk_bf16_f32 v58, v48, v49\n"
    "v_add_f32 v23, v23, v49\n"
    "v_mfma_f32_16x16x32_bf16 v[36:39], v[72:75], a[40:43], v[36:39]\n"
    "buffer_load_dwordx4 v17, s[40:43], s59 offen offset:1024 lds\n"
    "v_exp_f32 v50, v50\n"
    "v_mfma_f32_16x16x32_bf16 v[36:39], v[76:79], a[44:47], v[36:39]\n"
    "v_add_f32 v23, v23, v50\n"
    "v_mfma_f32_16x16x32_bf16 v[40:43], v[80:83], a[32:35], 0\n"
    "buffer_load_dwordx4 v18, s[40:43], s59 offen offset:2048 lds\n"
    "v_exp_f32 v51, v51\n"
    "v_mfma_f32_16x16x32_bf16 v[40:43], v[84:87], a[36:39], v[40:43]\n"
    "v_cvt_pk_bf16_f32 v59, v50, v51\n"
    "v_add_f32 v23, v23, v51\n"
    "v_mfma_f32_16x16x32_bf16 v[40:43], v[88:91], a[40:43], v[40:43]\n"
    "buffer_load_dwordx4 v19, s[40:43], s59 offen offset:3072 lds\n"
    "v_exp_f32 v36, v36\n"
    "v_mfma_f32_16x16x32_bf16 v[40:43], v[92:95], a[44:47], v[40:43]\n"
    "v_add_f32 v24, v24, v36\n"
    "s_mov_b32 m0, s78\n"
    "v_mfma_f32_16x16x32_bf16 v[136:139], v[96:99], v[56:59], 0\n"
    "buffer_load_dwordx4 v20, s[44:47], s58 offen offset:0 lds\n"
    "v_exp_f32 v37, v37\n"
    "v_mfma_f32_16x16x32_bf16 v[140:143], v[100:103], v[56:59], 0\n"
    "v_cvt_pk_bf16_f32 v52, v36, v37\n"
    "v_add_f32 v24, v24, v37\n"
    "v_mfma_f32_16x16x32_bf16 a[120:123], v[104:107], v[56:59], 0\n"
    "buffer_load_dwordx4 v21, s[44:47], s58 offen offset:1024 lds\n"
    "v_exp_f32 v38, v38\n"
    "v_mfma_f32_16x16x32_bf16 a[124:127], v[108:111], v[56:59], 0\n"
    "v_add_f32 v24, v24, v38\n"
    "v_mfma_f32_16x16x32_bf16 a[128:131], v[112:115], v[56:59], 0\n"
    "buffer_load_dwordx4 v20, s[44:47], s58 offen offset:2048 lds\n"
    "v_exp_f32 v39, v39\n"
    "v_mfma_f32_16x16x32_bf16 a[132:135], v[116:119], v[56:59], 0\n"
    "v_cvt_pk_bf16_f32 v53, v38, v39\n"
    "v_add_f32 v24, v24, v39\n"
    "v_mfma_f32_16x16x32_bf16 a[136:139], v[120:123], v[56:59], 0\n"
    "buffer_load_dwordx4 v21, s[44:47], s58 offen offset:3072 lds\n"
    "v_exp_f32 v40, v40\n"
    "v_mfma_f32_16x16x32_bf16 a[140:143], v[124:127], v[56:59], 0\n"
    "v_add_f32 v24, v24, v40\n"
    "s_mov_b32 m0, s79\n"
    "v_mfma_f32_16x16x32_bf16 v[44:47], v[64:67], a[48:51], 0\n"
    "buffer_load_dwordx4 v20, s[44:47], s59 offen offset:0 lds\n"
    "v_exp_f32 v41, v41\n"
    "v_mfma_f32_16x16x32_bf16 v[44:47], v[68:71], a[52:55], v[44:47]\n"
    "v_cvt_pk_bf16_f32 v54, v40, v41\n"
    "v_add_f32 v24, v24, v41\n"
    "v_mfma_f32_16x16x32_bf16 v[44:47], v[72:75], a[56:59], v[44:47]\n"
    "buffer_load_dwordx4 v21, s[44:47], s59 offen offset:1024 lds\n"
    "v_exp_f32 v42, v42\n"
    "v_mfma_f32_16x16x32_bf16 v[44:47], v[76:79], a[60:63], v[44:47]\n"
    "v_add_f32 v24, v24, v42\n"
    "v_mfma_f32_16x16x32_bf16 v[48:51], v[80:83], a[48:51], 0\n"
    "buffer_load_dwordx4 v20, s[44:47], s59 offen offset:2048 lds\n"
    "v_exp_f32 v43, v43\n"
    "s_waitcnt vmcnt(16)\n"
    "v_lshlrev_b32 v200, 16, v192\n"
    "v_and_b32 v201, 0xffff0000, v192\n"
    "v_lshlrev_b32 v202, 16, v193\n"
    "v_and_b32 v203, 0xffff0000, v193\n"
    "v_lshlrev_b32 v204, 16, v194\n"
    "v_and_b32 v205, 0xffff0000, v194\n"
    "v_mfma_f32_16x16x32_bf16 v[48:51], v[84:87], a[52:55], v[48:51]\n"
    "v_cvt_pk_bf16_f32 v55, v42, v43\n"
    "v_add_f32 v24, v24, v43\n"
    "v_lshlrev_b32 v206, 16, v195\n"
    "v_and_b32 v207, 0xffff0000, v195\n"
    "v_pk_mul_f32 v[200:201], v[200:201], s[90:91]\n"
    "v_pk_mul_f32 v[202:203], v[202:203], s[90:91]\n"
    "v_pk_mul_f32 v[204:205], v[204:205], s[90:91]\n"
    "v_pk_mul_f32 v[206:207], v[206:207], s[90:91]\n"
    "v_mfma_f32_16x16x32_bf16 v[48:51], v[88:91], a[56:59], v[48:51]\n"
    "buffer_load_dwordx4 v21, s[44:47], s59 offen offset:3072 lds\n"
    "v_exp_f32 v44, v44\n"
    "v_cvt_pk_bf16_f32 v192, v200, v201\n"
    "v_cvt_pk_bf16_f32 v193, v202, v203\n"
    "v_cvt_pk_bf16_f32 v194, v204, v205\n"
    "v_cvt_pk_bf16_f32 v195, v206, v207\n"
    "ds_write_b128 v212, v[192:195] offset:16384\n"
    "v_mfma_f32_16x16x32_bf16 v[48:51], v[92:95], a[60:63], v[48:51]\n"
    "v_add_f32 v25, v25, v44\n"
    "s_waitcnt vmcnt(16)\n"
    "v_lshlrev_b32 v200, 16, v196\n"
    "v_and_b32 v201, 0xffff0000, v196\n"
    "v_lshlrev_b32 v202, 16, v197\n"
    "v_and_b32 v203, 0xffff0000, v197\n"
    "v_lshlrev_b32 v204, 16, v198\n"
    "v_and_b32 v205, 0xffff0000, v198\n"
    "v_mfma_f32_16x16x32_bf16 v[144:147], v[96:99], v[52:55], 0\n"
    "v_exp_f32 v45, v45\n"
    "v_lshlrev_b32 v206, 16, v199\n"
    "v_and_b32 v207, 0xffff0000, v199\n"
    "v_pk_mul_f32 v[200:201], v[200:201], s[90:91]\n"
    "v_pk_mul_f32 v[202:203], v[202:203], s[90:91]\n"
    "v_pk_mul_f32 v[204:205], v[204:205], s[90:91]\n"
    "v_pk_mul_f32 v[206:207], v[206:207], s[90:91]\n"
    "v_mfma_f32_16x16x32_bf16 v[148:151], v[100:103], v[52:55], 0\n"
    "v_cvt_pk_bf16_f32 v56, v44, v45\n"
    "v_add_f32 v25, v25, v45\n"
    "v_cvt_pk_bf16_f32 v196, v200, v201\n"
    "v_cvt_pk_bf16_f32 v197, v202, v203\n"
    "v_cvt_pk_bf16_f32 v198, v204, v205\n"
    "v_cvt_pk_bf16_f32 v199, v206, v207\n"
    "ds_write_b128 v213, v[196:199] offset:17408\n"
    "v_mfma_f32_16x16x32_bf16 a[144:147], v[104:107], v[52:55], 0\n"
    "v_exp_f32 v46, v46\n"
    "s_mov_b32 m0, s72\n"
    "s_waitcnt lgkmcnt(0)\n"
    "s_barrier\n"
    "v_mfma_f32_16x16x32_bf16 a[148:151], v[108:111], v[52:55], 0\n"
    "ds_read_b128 a[64:67], v208 offset:16384\n"
    "ds_read_b128 a[68:71], v209 offset:16384\n"
    "buffer_load_dwordx4 v16, s[40:43], s60 offen offset:0 lds\n"
    "v_add_f32 v25, v25, v46\n"
    "v_mfma_f32_16x16x32_bf16 a[152:155], v[112:115], v[52:55], 0\n"
    "ds_read_b128 a[72:75], v210 offset:16384\n"
    "ds_read_b128 a[76:79], v211 offset:16384\n"
    "v_exp_f32 v47, v47\n"
    "v_mfma_f32_16x16x32_bf16 a[156:159], v[116:119], v[52:55], 0\n"
    "ds_read_b128 a[80:83], v208 offset:20480\n"
    "ds_read_b128 a[84:87], v209 offset:20480\n"
    "v_cvt_pk_bf16_f32 v57, v46, v47\n"
    "v_add_f32 v25, v25, v47\n"
    "v_mfma_f32_16x16x32_bf16 a[160:163], v[120:123], v[52:55], 0\n"
    "ds_read_b128 a[88:91], v210 offset:20480\n"
    "ds_read_b128 a[92:95], v211 offset:20480\n"
    "v_exp_f32 v48, v48\n"
    "v_mfma_f32_16x16x32_bf16 a[164:167], v[124:127], v[52:55], 0\n"
    "buffer_load_dwordx4 v17, s[40:43], s60 offen offset:1024 lds\n"
    "v_add_f32 v25, v25, v48\n"
    "s_waitcnt lgkmcnt(7)\n"
    "v_mfma_f32_16x16x32_bf16 v[36:39], v[64:67], a[64:67], 0\n"
    "v_exp_f32 v49, v49\n"
    "s_waitcnt lgkmcnt(6)\n"
    "v_mfma_f32_16x16x32_bf16 v[36:39], v[68:71], a[68:71], v[36:39]\n"
    "v_cvt_pk_bf16_f32 v58, v48, v49\n"
    "v_add_f32 v25, v25, v49\n"
    "s_waitcnt lgkmcnt(5)\n"
    "v_mfma_f32_16x16x32_bf16 v[36:39], v[72:75], a[72:75], v[36:39]\n"
    "v_exp_f32 v50, v50\n"
    "s_waitcnt lgkmcnt(4)\n"
    "v_mfma_f32_16x16x32_bf16 v[36:39], v[76:79], a[76:79], v[36:39]\n"
    "buffer_load_dwordx4 v18, s[40:43], s60 offen offset:2048 lds\n"
    "v_add_f32 v25, v25, v50\n"
    "v_mfma_f32_16x16x32_bf16 v[40:43], v[80:83], a[64:67], 0\n"
    "v_exp_f32 v51, v51\n"
    "v_mfma_f32_16x16x32_bf16 v[40:43], v[84:87], a[68:71], v[40:43]\n"
    "v_cvt_pk_bf16_f32 v59, v50, v51\n"
    "v_add_f32 v25, v25, v51\n"
    "v_mfma_f32_16x16x32_bf16 v[40:43], v[88:91], a[72:75], v[40:43]\n"
    "v_exp_f32 v36, v36\n"
    "v_mfma_f32_16x16x32_bf16 v[40:43], v[92:95], a[76:79], v[40:43]\n"
    "buffer_load_dwordx4 v19, s[40:43], s60 offen offset:3072 lds\n"
    "v_add_f32 v26, v26, v36\n"
    "v_mfma_f32_16x16x32_bf16 v[152:155], v[96:99], v[56:59], 0\n"
    "v_exp_f32 v37, v37\n"
    "s_mov_b32 m0, s73\n"
    "v_mfma_f32_16x16x32_bf16 v[156:159], v[100:103], v[56:59], 0\n"
    "v_cvt_pk_bf16_f32 v52, v36, v37\n"
    "v_add_f32 v26, v26, v37\n"
    "v_mfma_f32_16x16x32_bf16 a[168:171], v[104:107], v[56:59], 0\n"
    "v_exp_f32 v38, v38\n"
    "v_mfma_f32_16x16x32_bf16 a[172:175], v[108:111], v[56:59], 0\n"
    "buffer_load_dwordx4 v16, s[40:43], s61 offen offset:0 lds\n"
    "v_add_f32 v26, v26, v38\n"
    "v_mfma_f32_16x16x32_bf16 a[176:179], v[112:115], v[56:59], 0\n"
    "v_exp_f32 v39, v39\n"
    "v_mfma_f32_16x16x32_bf16 a[180:183], v[116:119], v[56:59], 0\n"
    "v_cvt_pk_bf16_f32 v53, v38, v39\n"
    "v_add_f32 v26, v26, v39\n"
    "v_mfma_f32_16x16x32_bf16 a[184:187], v[120:123], v[56:59], 0\n"
    "v_exp_f32 v40, v40\n"
    "v_mfma_f32_16x16x32_bf16 a[188:191], v[124:127], v[56:59], 0\n"
    "buffer_load_dwordx4 v17, s[40:43], s61 offen offset:1024 lds\n"
    "v_add_f32 v26, v26, v40\n"
    "s_waitcnt lgkmcnt(3)\n"
    "v_mfma_f32_16x16x32_bf16 v[44:47], v[64:67], a[80:83], 0\n"
    "s_waitcnt vmcnt(14)\n"
    "ds_read_b128 v[64:67], v4 offset:16384\n"
    "v_exp_f32 v41, v41\n"
    "s_waitcnt lgkmcnt(3)\n"
    "v_mfma_f32_16x16x32_bf16 v[44:47], v[68:71], a[84:87], v[44:47]\n"
    "ds_read_b128 v[68:71], v5 offset:16384\n"
    "v_cvt_pk_bf16_f32 v54, v40, v41\n"
    "v_add_f32 v26, v26, v41\n"
    "s_waitcnt lgkmcnt(3)\n"
    "v_mfma_f32_16x16x32_bf16 v[44:47], v[72:75], a[88:91], v[44:47]\n"
    "ds_read_b128 v[72:75], v6 offset:16384\n"
    "v_exp_f32 v42, v42\n"
    "s_waitcnt lgkmcnt(3)\n"
    "v_mfma_f32_16x16x32_bf16 v[44:47], v[76:79], a[92:95], v[44:47]\n"
    "ds_read_b128 v[76:79], v7 offset:16384\n"
    "v_add_f32 v26, v26, v42\n"
    "v_mfma_f32_16x16x32_bf16 v[48:51], v[80:83], a[80:83], 0\n"
    "ds_read_b128 v[80:83], v4 offset:20480\n"
    "v_exp_f32 v43, v43\n"
    "v_mfma_f32_16x16x32_bf16 v[48:51], v[84:87], a[84:87], v[48:51]\n"
    "ds_read_b128 v[84:87], v5 offset:20480\n"
    "v_cvt_pk_bf16_f32 v55, v42, v43\n"
    "v_add_f32 v26, v26, v43\n"
    "v_mfma_f32_16x16x32_bf16 v[48:51], v[88:91], a[88:91], v[48:51]\n"
    "ds_read_b128 v[88:91], v6 offset:20480\n"
    "v_exp_f32 v44, v44\n"
    "v_mfma_f32_16x16x32_bf16 v[48:51], v[92:95], a[92:95], v[48:51]\n"
    "ds_read_b128 v[92:95], v7 offset:20480\n"
    "v_add_f32 v27, v27, v44\n"
    "v_mfma_f32_16x16x32_bf16 v[160:163], v[96:99], v[52:55], 0\n"
    "v_exp_f32 v45, v45\n"
    "v_mfma_f32_16x16x32_bf16 v[164:167], v[100:103], v[52:55], 0\n"
    "v_cvt_pk_bf16_f32 v56, v44, v45\n"
    "v_add_f32 v27, v27, v45\n"
    "v_mfma_f32_16x16x32_bf16 a[192:195], v[104:107], v[52:55], 0\n"
    "v_exp_f32 v46, v46\n"
    "v_mfma_f32_16x16x32_bf16 a[196:199], v[108:111], v[52:55], 0\n"
    "buffer_load_dwordx4 v18, s[40:43], s61 offen offset:2048 lds\n"
    "v_add_f32 v27, v27, v46\n"
    "v_mfma_f32_16x16x32_bf16 a[200:203], v[112:115], v[52:55], 0\n"
    "buffer_load_dwordx4 v19, s[40:43], s61 offen offset:3072 lds\n"
    "v_exp_f32 v47, v47\n"
    "v_mfma_f32_16x16x32_bf16 a[204:207], v[116:119], v[52:55], 0\n"
    "v_cvt_pk_bf16_f32 v57, v46, v47\n"
    "v_add_f32 v27, v27, v47\n"
    "s_mov_b32 m0, s74\n"
    "v_mfma_f32_16x16x32_bf16 a[208:211], v[120:123], v[52:55], 0\n"
    "buffer_load_dwordx4 v20, s[44:47], s60 offen offset:0 lds\n"
    "v_exp_f32 v48, v48\n"
    "v_mfma_f32_16x16x32_bf16 a[212:215], v[124:127], v[52:55], 0\n"
    "v_add_f32 v27, v27, v48\n"
    "s_waitcnt lgkmcnt(4)\n"
    "v_mfma_f32_16x16x32_bf16 v[36:39], v[64:67], a[0:3], 0\n"
    "v_exp_f32 v49, v49\n"
    "v_mfma_f32_16x16x32_bf16 v[36:39], v[68:71], a[4:7], v[36:39]\n"
    "v_cvt_pk_bf16_f32 v58, v48, v49\n"
    "v_add_f32 v27, v27, v49\n"
    "v_mfma_f32_16x16x32_bf16 v[36:39], v[72:75], a[8:11], v[36:39]\n"
    "v_exp_f32 v50, v50\n"
    "v_mfma_f32_16x16x32_bf16 v[36:39], v[76:79], a[12:15], v[36:39]\n"
    "v_add_f32 v27, v27, v50\n"
    "s_waitcnt lgkmcnt(0)\n"
    "v_mfma_f32_16x16x32_bf16 v[40:43], v[80:83], a[0:3], 0\n"
    "v_exp_f32 v51, v51\n"
    "v_mfma_f32_16x16x32_bf16 v[40:43], v[84:87], a[4:7], v[40:43]\n"
    "v_cvt_pk_bf16_f32 v59, v50, v51\n"
    "v_add_f32 v27, v27, v51\n"
    "v_mfma_f32_16x16x32_bf16 v[40:43], v[88:91], a[8:11], v[40:43]\n"
    "v_exp_f32 v36, v36\n"
    "v_mfma_f32_16x16x32_bf16 v[40:43], v[92:95], a[12:15], v[40:43]\n"
    "buffer_load_dwordx4 v21, s[44:47], s60 offen offset:1024 lds\n"
    "v_add_f32 v22, v22, v36\n"
    "v_mfma_f32_16x16x32_bf16 v[168:171], v[96:99], v[56:59], 0\n"
    "s_waitcnt vmcnt(10)\n"
    "ds_read_b64_tr_b16 v[96:97], v8 offset:24576\n"
    "ds_read_b64_tr_b16 v[98:99], v8 offset:28672\n"
    "v_exp_f32 v37, v37\n"
    "v_mfma_f32_16x16x32_bf16 v[172:175], v[100:103], v[56:59], 0\n"
    "ds_read_b64_tr_b16 v[100:101], v9 offset:24576\n"
    "ds_read_b64_tr_b16 v[102:103], v9 offset:28672\n"
    "v_cvt_pk_bf16_f32 v52, v36, v37\n"
    "v_add_f32 v22, v22, v37\n"
    "v_mfma_f32_16x16x32_bf16 a[216:219], v[104:107], v[56:59], 0\n"
    "ds_read_b64_tr_b16 v[104:105], v10 offset:24576\n"
    "ds_read_b64_tr_b16 v[106:107], v10 offset:28672\n"
    "v_exp_f32 v38, v38\n"
    "v_mfma_f32_16x16x32_bf16 a[220:223], v[108:111], v[56:59], 0\n"
    "ds_read_b64_tr_b16 v[108:109], v11 offset:24576\n"
    "ds_read_b64_tr_b16 v[110:111], v11 offset:28672\n"
    "buffer_load_dwordx4 v20, s[44:47], s60 offen offset:2048 lds\n"
    "v_add_f32 v22, v22, v38\n"
    "v_mfma_f32_16x16x32_bf16 a[224:227], v[112:115], v[56:59], 0\n"
    "ds_read_b64_tr_b16 v[112:113], v12 offset:24576\n"
    "ds_read_b64_tr_b16 v[114:115], v12 offset:28672\n"
    "v_exp_f32 v39, v39\n"
    "v_mfma_f32_16x16x32_bf16 a[228:231], v[116:119], v[56:59], 0\n"
    "ds_read_b64_tr_b16 v[116:117], v13 offset:24576\n"
    "ds_read_b64_tr_b16 v[118:119], v13 offset:28672\n"
    "v_cvt_pk_bf16_f32 v53, v38, v39\n"
    "v_add_f32 v22, v22, v39\n"
    "v_mfma_f32_16x16x32_bf16 a[232:235], v[120:123], v[56:59], 0\n"
    "ds_read_b64_tr_b16 v[120:121], v14 offset:24576\n"
    "ds_read_b64_tr_b16 v[122:123], v14 offset:28672\n"
    "v_exp_f32 v40, v40\n"
    "v_mfma_f32_16x16x32_bf16 a[236:239], v[124:127], v[56:59], 0\n"
    "ds_read_b64_tr_b16 v[124:125], v15 offset:24576\n"
    "ds_read_b64_tr_b16 v[126:127], v15 offset:28672\n"
    "v_add_f32 v22, v22, v40\n"
    "v_mfma_f32_16x16x32_bf16 v[44:47], v[64:67], a[16:19], 0\n"
    "buffer_load_dwordx4 v21, s[44:47], s60 offen offset:3072 lds\n"
    "v_exp_f32 v41, v41\n"
    "v_mfma_f32_16x16x32_bf16 v[44:47], v[68:71], a[20:23], v[44:47]\n"
    "v_cvt_pk_bf16_f32 v54, v40, v41\n"
    "v_add_f32 v22, v22, v41\n"
    "v_mfma_f32_16x16x32_bf16 v[44:47], v[72:75], a[24:27], v[44:47]\n"
    "v_exp_f32 v42, v42\n"
    "v_mfma_f32_16x16x32_bf16 v[44:47], v[76:79], a[28:31], v[44:47]\n"
    "v_add_f32 v22, v22, v42\n"
    "s_mov_b32 m0, s75\n"
    "v_mfma_f32_16x16x32_bf16 v[48:51], v[80:83], a[16:19], 0\n"
    "buffer_load_dwordx4 v20, s[44:47], s61 offen offset:0 lds\n"
    "v_exp_f32 v43, v43\n"
    "v_mfma_f32_16x16x32_bf16 v[48:51], v[84:87], a[20:23], v[48:51]\n"
    "buffer_load_dwordx4 v21, s[44:47], s61 offen offset:1024 lds\n"
    "v_cvt_pk_bf16_f32 v55, v42, v43\n"
    "v_add_f32 v22, v22, v43\n"
    "v_mfma_f32_16x16x32_bf16 v[48:51], v[88:91], a[24:27], v[48:51]\n"
    "v_exp_f32 v44, v44\n"
    "v_mfma_f32_16x16x32_bf16 v[48:51], v[92:95], a[28:31], v[48:51]\n"
    "v_add_f32 v23, v23, v44\n"
    "s_waitcnt lgkmcnt(8)\n"
    "v_mfma_f32_16x16x32_bf16 v[128:131], v[96:99], v[52:55], v[128:131]\n"
    "v_exp_f32 v45, v45\n"
    "v_mfma_f32_16x16x32_bf16 v[132:135], v[100:103], v[52:55], v[132:135]\n"
    "v_cvt_pk_bf16_f32 v56, v44, v45\n"
    "v_add_f32 v23, v23, v45\n"
    "v_mfma_f32_16x16x32_bf16 a[96:99], v[104:107], v[52:55], a[96:99]\n"
    "v_exp_f32 v46, v46\n"
    "v_mfma_f32_16x16x32_bf16 a[100:103], v[108:111], v[52:55], a[100:103]\n"
    "v_add_f32 v23, v23, v46\n"
    "s_waitcnt lgkmcnt(0)\n"
    "v_mfma_f32_16x16x32_bf16 a[104:107], v[112:115], v[52:55], a[104:107]\n"
    "v_exp_f32 v47, v47\n"
    "v_mfma_f32_16x16x32_bf16 a[108:111], v[116:119], v[52:55], a[108:111]\n"
    "v_cvt_pk_bf16_f32 v57, v46, v47\n"
    "v_add_f32 v23, v23, v47\n"
    "v_mfma_f32_16x16x32_bf16 a[112:115], v[120:123], v[52:55], a[112:115]\n"
    "v_exp_f32 v48, v48\n"
    "v_mfma_f32_16x16x32_bf16 a[116:119], v[124:127], v[52:55], a[116:119]\n"
    "buffer_load_dwordx4 v20, s[44:47], s61 offen offset:2048 lds\n"
    "v_add_f32 v23, v23, v48\n"
    "v_mfma_f32_16x16x32_bf16 v[36:39], v[64:67], a[32:35], 0\n"
    "v_exp_f32 v49, v49\n"
    "v_mfma_f32_16x16x32_bf16 v[36:39], v[68:71], a[36:39], v[36:39]\n"
    "v_cvt_pk_bf16_f32 v58, v48, v49\n"
    "v_add_f32 v23, v23, v49\n"
    "v_mfma_f32_16x16x32_bf16 v[36:39], v[72:75], a[40:43], v[36:39]\n"
    "v_exp_f32 v50, v50\n"
    "v_mfma_f32_16x16x32_bf16 v[36:39], v[76:79], a[44:47], v[36:39]\n"
    "buffer_load_dwordx4 v21, s[44:47], s61 offen offset:3072 lds\n"
    "v_add_f32 v23, v23, v50\n"
    "v_mfma_f32_16x16x32_bf16 v[40:43], v[80:83], a[32:35], 0\n"
    "v_exp_f32 v51, v51\n"
    "s_mov_b32 m0, s76\n"
    "v_mfma_f32_16x16x32_bf16 v[40:43], v[84:87], a[36:39], v[40:43]\n"
    "v_cvt_pk_bf16_f32 v59, v50, v51\n"
    "v_add_f32 v23, v23, v51\n"
    "v_mfma_f32_16x16x32_bf16 v[40:43], v[88:91], a[40:43], v[40:43]\n"
    "v_exp_f32 v36, v36\n"
    "v_mfma_f32_16x16x32_bf16 v[40:43], v[92:95], a[44:47], v[40:43]\n"
    "buffer_load_dwordx4 v16, s[40:43], s62 offen offset:0 lds\n"
    "v_add_f32 v24, v24, v36\n"
    "v_mfma_f32_16x16x32_bf16 v[136:139], v[96:99], v[56:59], v[136:139]\n"
    "v_exp_f32 v37, v37\n"
    "v_mfma_f32_16x16x32_bf16 v[140:143], v[100:103], v[56:59], v[140:143]\n"
    "v_cvt_pk_bf16_f32 v52, v36, v37\n"
    "v_add_f32 v24, v24, v37\n"
    "v_mfma_f32_16x16x32_bf16 a[120:123], v[104:107], v[56:59], a[120:123]\n"
    "v_exp_f32 v38, v38\n"
    "v_mfma_f32_16x16x32_bf16 a[124:127], v[108:111], v[56:59], a[124:127]\n"
    "buffer_load_dwordx4 v17, s[40:43], s62 offen offset:1024 lds\n"
    "v_add_f32 v24, v24, v38\n"
    "v_mfma_f32_16x16x32_bf16 a[128:131], v[112:115], v[56:59], a[128:131]\n"
    "v_exp_f32 v39, v39\n"
    "v_mfma_f32_16x16x32_bf16 a[132:135], v[116:119], v[56:59], a[132:135]\n"
    "v_cvt_pk_bf16_f32 v53, v38, v39\n"
    "v_add_f32 v24, v24, v39\n"
    "v_mfma_f32_16x16x32_bf16 a[136:139], v[120:123], v[56:59], a[136:139]\n"
    "v_exp_f32 v40, v40\n"
    "v_mfma_f32_16x16x32_bf16 a[140:143], v[124:127], v[56:59], a[140:143]\n"
    "buffer_load_dwordx4 v18, s[40:43], s62 offen offset:2048 lds\n"
    "v_add_f32 v24, v24, v40\n"
    "v_mfma_f32_16x16x32_bf16 v[44:47], v[64:67], a[48:51], 0\n"
    "v_exp_f32 v41, v41\n"
    "v_mfma_f32_16x16x32_bf16 v[44:47], v[68:71], a[52:55], v[44:47]\n"
    "v_cvt_pk_bf16_f32 v54, v40, v41\n"
    "v_add_f32 v24, v24, v41\n"
    "v_mfma_f32_16x16x32_bf16 v[44:47], v[72:75], a[56:59], v[44:47]\n"
    "v_exp_f32 v42, v42\n"
    "v_mfma_f32_16x16x32_bf16 v[44:47], v[76:79], a[60:63], v[44:47]\n"
    "buffer_load_dwordx4 v19, s[40:43], s62 offen offset:3072 lds\n"
    "v_add_f32 v24, v24, v42\n"
    "v_mfma_f32_16x16x32_bf16 v[48:51], v[80:83], a[48:51], 0\n"
    "v_exp_f32 v43, v43\n"
    "s_mov_b32 m0, s77\n"
    "v_mfma_f32_16x16x32_bf16 v[48:51], v[84:87], a[52:55], v[48:51]\n"
    "v_cvt_pk_bf16_f32 v55, v42, v43\n"
    "v_add_f32 v24, v24, v43\n"
    "v_mfma_f32_16x16x32_bf16 v[48:51], v[88:91], a[56:59], v[48:51]\n"
    "v_exp_f32 v44, v44\n"
    "v_mfma_f32_16x16x32_bf16 v[48:51], v[92:95], a[60:63], v[48:51]\n"
    "buffer_load_dwordx4 v16, s[40:43], s63 offen offset:0 lds\n"
    "v_add_f32 v25, v25, v44\n"
    "v_mfma_f32_16x16x32_bf16 v[144:147], v[96:99], v[52:55], v[144:147]\n"
    "v_exp_f32 v45, v45\n"
    "v_mfma_f32_16x16x32_bf16 v[148:151], v[100:103], v[52:55], v[148:151]\n"
    "v_cvt_pk_bf16_f32 v56, v44, v45\n"
    "v_add_f32 v25, v25, v45\n"
    "v_mfma_f32_16x16x32_bf16 a[144:147], v[104:107], v[52:55], a[144:147]\n"
    "v_exp_f32 v46, v46\n"
    "v_mfma_f32_16x16x32_bf16 a[148:151], v[108:111], v[52:55], a[148:151]\n"
    "buffer_load_dwordx4 v17, s[40:43], s63 offen offset:1024 lds\n"
    "v_add_f32 v25, v25, v46\n"
    "v_mfma_f32_16x16x32_bf16 a[152:155], v[112:115], v[52:55], a[152:155]\n"
    "v_exp_f32 v47, v47\n"
    "v_mfma_f32_16x16x32_bf16 a[156:159], v[116:119], v[52:55], a[156:159]\n"
    "v_cvt_pk_bf16_f32 v57, v46, v47\n"
    "v_add_f32 v25, v25, v47\n"
    "v_mfma_f32_16x16x32_bf16 a[160:163], v[120:123], v[52:55], a[160:163]\n"
    "v_exp_f32 v48, v48\n"
    "v_mfma_f32_16x16x32_bf16 a[164:167], v[124:127], v[52:55], a[164:167]\n"
    "buffer_load_dwordx4 v18, s[40:43], s63 offen offset:2048 lds\n"
    "v_add_f32 v25, v25, v48\n"
    "v_mfma_f32_16x16x32_bf16 v[36:39], v[64:67], a[64:67], 0\n"
    "v_exp_f32 v49, v49\n"
    "v_mfma_f32_16x16x32_bf16 v[36:39], v[68:71], a[68:71], v[36:39]\n"
    "v_cvt_pk_bf16_f32 v58, v48, v49\n"
    "v_add_f32 v25, v25, v49\n"
    "v_mfma_f32_16x16x32_bf16 v[36:39], v[72:75], a[72:75], v[36:39]\n"
    "v_exp_f32 v50, v50\n"
    "v_mfma_f32_16x16x32_bf16 v[36:39], v[76:79], a[76:79], v[36:39]\n"
    "buffer_load_dwordx4 v19, s[40:43], s63 offen offset:3072 lds\n"
    "v_add_f32 v25, v25, v50\n"
    "v_mfma_f32_16x16x32_bf16 v[40:43], v[80:83], a[64:67], 0\n"
    "v_exp_f32 v51, v51\n"
    "s_mov_b32 m0, s78\n"
    "v_mfma_f32_16x16x32_bf16 v[40:43], v[84:87], a[68:71], v[40:43]\n"
    "v_cvt_pk_bf16_f32 v59, v50, v51\n"
    "v_add_f32 v25, v25, v51\n"
    "v_mfma_f32_16x16x32_bf16 v[40:43], v[88:91], a[72:75], v[40:43]\n"
    "v_exp_f32 v36, v36\n"
    "v_mfma_f32_16x16x32_bf16 v[40:43], v[92:95], a[76:79], v[40:43]\n"
    "buffer_load_dwordx4 v20, s[44:47], s62 offen offset:0 lds\n"
    "v_add_f32 v26, v26, v36\n"
    "v_mfma_f32_16x16x32_bf16 v[152:155], v[96:99], v[56:59], v[152:155]\n"
    "v_exp_f32 v37, v37\n"
    "v_mfma_f32_16x16x32_bf16 v[156:159], v[100:103], v[56:59], v[156:159]\n"
    "v_cvt_pk_bf16_f32 v52, v36, v37\n"
    "v_add_f32 v26, v26, v37\n"
    "v_mfma_f32_16x16x32_bf16 a[168:171], v[104:107], v[56:59], a[168:171]\n"
    "v_exp_f32 v38, v38\n"
    "v_mfma_f32_16x16x32_bf16 a[172:175], v[108:111], v[56:59], a[172:175]\n"
    "buffer_load_dwordx4 v21, s[44:47], s62 offen offset:1024 lds\n"
    "v_add_f32 v26, v26, v38\n"
    "v_mfma_f32_16x16x32_bf16 a[176:179], v[112:115], v[56:59], a[176:179]\n"
    "v_exp_f32 v39, v39\n"
    "v_mfma_f32_16x16x32_bf16 a[180:183], v[116:119], v[56:59], a[180:183]\n"
    "v_cvt_pk_bf16_f32 v53, v38, v39\n"
    "v_add_f32 v26, v26, v39\n"
    "v_mfma_f32_16x16x32_bf16 a[184:187], v[120:123], v[56:59], a[184:187]\n"
    "v_exp_f32 v40, v40\n"
    "v_mfma_f32_16x16x32_bf16 a[188:191], v[124:127], v[56:59], a[188:191]\n"
    "buffer_load_dwordx4 v20, s[44:47], s62 offen offset:2048 lds\n"
    "v_add_f32 v26, v26, v40\n"
    "v_mfma_f32_16x16x32_bf16 v[44:47], v[64:67], a[80:83], 0\n"
    "s_waitcnt vmcnt(19)\n"
    "ds_read_b128 v[64:67], v4 offset:0\n"
    "v_exp_f32 v41, v41\n"
    "v_mfma_f32_16x16x32_bf16 v[44:47], v[68:71], a[84:87], v[44:47]\n"
    "ds_read_b128 v[68:71], v5 offset:0\n"
    "v_cvt_pk_bf16_f32 v54, v40, v41\n"
    "v_add_f32 v26, v26, v41\n"
    "v_mfma_f32_16x16x32_bf16 v[44:47], v[72:75], a[88:91], v[44:47]\n"
    "ds_read_b128 v[72:75], v6 offset:0\n"
    "v_exp_f32 v42, v42\n"
    "v_mfma_f32_16x16x32_bf16 v[44:47], v[76:79], a[92:95], v[44:47]\n"
    "ds_read_b128 v[76:79], v7 offset:0\n"
    "buffer_load_dwordx4 v21, s[44:47], s62 offen offset:3072 lds\n"
    "v_add_f32 v26, v26, v42\n"
    "v_mfma_f32_16x16x32_bf16 v[48:51], v[80:83], a[80:83], 0\n"
    "ds_read_b128 v[80:83], v4 offset:4096\n"
    "v_exp_f32 v43, v43\n"
    "s_mov_b32 m0, s79\n"
    "v_mfma_f32_16x16x32_bf16 v[48:51], v[84:87], a[84:87], v[48:51]\n"
    "ds_read_b128 v[84:87], v5 offset:4096\n"
    "v_cvt_pk_bf16_f32 v55, v42, v43\n"
    "v_add_f32 v26, v26, v43\n"
    "v_mfma_f32_16x16x32_bf16 v[48:51], v[88:91], a[88:91], v[48:51]\n"
    "ds_read_b128 v[88:91], v6 offset:4096\n"
    "v_exp_f32 v44, v44\n"
    "v_mfma_f32_16x16x32_bf16 v[48:51], v[92:95], a[92:95], v[48:51]\n"
    "ds_read_b128 v[92:95], v7 offset:4096\n"
    "v_add_f32 v27, v27, v44\n"
    "v_mfma_f32_16x16x32_bf16 v[160:163], v[96:99], v[52:55], v[160:163]\n"
    "v_exp_f32 v45, v45\n"
    "v_mfma_f32_16x16x32_bf16 v[164:167], v[100:103], v[52:55], v[164:167]\n"
    "v_cvt_pk_bf16_f32 v56, v44, v45\n"
    "v_add_f32 v27, v27, v45\n"
    "v_mfma_f32_16x16x32_bf16 a[192:195], v[104:107], v[52:55], a[192:195]\n"
    "v_exp_f32 v46, v46\n"
    "v_mfma_f32_16x16x32_bf16 a[196:199], v[108:111], v[52:55], a[196:199]\n"
    "buffer_load_dwordx4 v20, s[44:47], s63 offen offset:0 lds\n"
    "v_add_f32 v27, v27, v46\n"
    "v_mfma_f32_16x16x32_bf16 a[200:203], v[112:115], v[52:55], a[200:203]\n"
    "buffer_load_dwordx4 v21, s[44:47], s63 offen offset:1024 lds\n"
    "v_exp_f32 v47, v47\n"
    "v_mfma_f32_16x16x32_bf16 a[204:207], v[116:119], v[52:55], a[204:207]\n"
    "v_cvt_pk_bf16_f32 v57, v46, v47\n"
    "v_add_f32 v27, v27, v47\n"
    "v_mfma_f32_16x16x32_bf16 a[208:211], v[120:123], v[52:55], a[208:211]\n"
    "v_exp_f32 v48, v48\n"
    "v_mfma_f32_16x16x32_bf16 a[212:215], v[124:127], v[52:55], a[212:215]\n"
    "v_add_f32 v27, v27, v48\n"
    "s_waitcnt lgkmcnt(4)\n"
    "v_mfma_f32_16x16x32_bf16 v[36:39], v[64:67], a[0:3], 0\n"
    "v_exp_f32 v49, v49\n"
    "v_mfma_f32_16x16x32_bf16 v[36:39], v[68:71], a[4:7], v[36:39]\n"
    "v_cvt_pk_bf16_f32 v58, v48, v49\n"
    "v_add_f32 v27, v27, v49\n"
    "v_mfma_f32_16x16x32_bf16 v[36:39], v[72:75], a[8:11], v[36:39]\n"
    "v_exp_f32 v50, v50\n"
    "v_mfma_f32_16x16x32_bf16 v[36:39], v[76:79], a[12:15], v[36:39]\n"
    "v_add_f32 v27, v27, v50\n"
    "s_waitcnt lgkmcnt(0)\n"
    "v_mfma_f32_16x16x32_bf16 v[40:43], v[80:83], a[0:3], 0\n"
    "v_exp_f32 v51, v51\n"
    "v_mfma_f32_16x16x32_bf16 v[40:43], v[84:87], a[4:7], v[40:43]\n"
    "v_cvt_pk_bf16_f32 v59, v50, v51\n"
    "v_add_f32 v27, v27, v51\n"
    "v_mfma_f32_16x16x32_bf16 v[40:43], v[88:91], a[8:11], v[40:43]\n"
    "v_exp_f32 v36, v36\n"
    "v_mfma_f32_16x16x32_bf16 v[40:43], v[92:95], a[12:15], v[40:43]\n"
    "buffer_load_dwordx4 v20, s[44:47], s63 offen offset:2048 lds\n"
    "v_add_f32 v22, v22, v36\n"
    "v_mfma_f32_16x16x32_bf16 v[168:171], v[96:99], v[56:59], v[168:171]\n"
    "s_waitcnt vmcnt(15)\n"
    "ds_read_b64_tr_b16 v[96:97], v8 offset:8192\n"
    "ds_read_b64_tr_b16 v[98:99], v8 offset:12288\n"
    "v_exp_f32 v37, v37\n"
    "v_mfma_f32_16x16x32_bf16 v[172:175], v[100:103], v[56:59], v[172:175]\n"
    "ds_read_b64_tr_b16 v[100:101], v9 offset:8192\n"
    "ds_read_b64_tr_b16 v[102:103], v9 offset:12288\n"
    "v_cvt_pk_bf16_f32 v52, v36, v37\n"
    "v_add_f32 v22, v22, v37\n"
    "v_mfma_f32_16x16x32_bf16 a[216:219], v[104:107], v[56:59], a[216:219]\n"
    "ds_read_b64_tr_b16 v[104:105], v10 offset:8192\n"
    "ds_read_b64_tr_b16 v[106:107], v10 offset:12288\n"
    "v_exp_f32 v38, v38\n"
    "v_mfma_f32_16x16x32_bf16 a[220:223], v[108:111], v[56:59], a[220:223]\n"
    "ds_read_b64_tr_b16 v[108:109], v11 offset:8192\n"
    "ds_read_b64_tr_b16 v[110:111], v11 offset:12288\n"
    "buffer_load_dwordx4 v21, s[44:47], s63 offen offset:3072 lds\n"
    "v_add_f32 v22, v22, v38\n"
    "v_mfma_f32_16x16x32_bf16 a[224:227], v[112:115], v[56:59], a[224:227]\n"
    "ds_read_b64_tr_b16 v[112:113], v12 offset:8192\n"
    "ds_read_b64_tr_b16 v[114:115], v12 offset:12288\n"
    "v_exp_f32 v39, v39\n"
    "s_mov_b32 m0, s72\n"
    "v_mfma_f32_16x16x32_bf16 a[228:231], v[116:119], v[56:59], a[228:231]\n"
    "ds_read_b64_tr_b16 v[116:117], v13 offset:8192\n"
    "ds_read_b64_tr_b16 v[118:119], v13 offset:12288\n"
    "v_cvt_pk_bf16_f32 v53, v38, v39\n"
    "v_add_f32 v22, v22, v39\n"
    "v_mfma_f32_16x16x32_bf16 a[232:235], v[120:123], v[56:59], a[232:235]\n"
    "ds_read_b64_tr_b16 v[120:121], v14 offset:8192\n"
    "ds_read_b64_tr_b16 v[122:123], v14 offset:12288\n"
    "v_exp_f32 v40, v40\n"
    "v_mfma_f32_16x16x32_bf16 a[236:239], v[124:127], v[56:59], a[236:239]\n"
    "ds_read_b64_tr_b16 v[124:125], v15 offset:8192\n"
    "ds_read_b64_tr_b16 v[126:127], v15 offset:12288\n"
    "v_add_f32 v22, v22, v40\n"
    "v_mfma_f32_16x16x32_bf16 v[44:47], v[64:67], a[16:19], 0\n"
    "buffer_load_dwordx4 v16, s[40:43], s64 offen offset:0 lds\n"
    "v_exp_f32 v41, v41\n"
    "v_mfma_f32_16x16x32_bf16 v[44:47], v[68:71], a[20:23], v[44:47]\n"
    "v_cvt_pk_bf16_f32 v54, v40, v41\n"
    "v_add_f32 v22, v22, v41\n"
    "v_mfma_f32_16x16x32_bf16 v[44:47], v[72:75], a[24:27], v[44:47]\n"
    "v_exp_f32 v42, v42\n"
    "v_mfma_f32_16x16x32_bf16 v[44:47], v[76:79], a[28:31], v[44:47]\n"
    "buffer_load_dwordx4 v17, s[40:43], s64 offen offset:1024 lds\n"
    "v_add_f32 v22, v22, v42\n"
    "v_mfma_f32_16x16x32_bf16 v[48:51], v[80:83], a[16:19], 0\n"
    "buffer_load_dwordx4 v18, s[40:43], s64 offen offset:2048 lds\n"
    "v_exp_f32 v43, v43\n"
    "v_mfma_f32_16x16x32_bf16 v[48:51], v[84:87], a[20:23], v[48:51]\n"
    "v_cvt_pk_bf16_f32 v55, v42, v43\n"
    "v_add_f32 v22, v22, v43\n"
    "v_mfma_f32_16x16x32_bf16 v[48:51], v[88:91], a[24:27], v[48:51]\n"
    "v_exp_f32 v44, v44\n"
    "v_mfma_f32_16x16x32_bf16 v[48:51], v[92:95], a[28:31], v[48:51]\n"
    "v_add_f32 v23, v23, v44\n"
    "s_waitcnt lgkmcnt(8)\n"
    "v_mfma_f32_16x16x32_bf16 v[128:131], v[96:99], v[52:55], v[128:131]\n"
    "v_exp_f32 v45, v45\n"
    "v_mfma_f32_16x16x32_bf16 v[132:135], v[100:103], v[52:55], v[132:135]\n"
    "v_cvt_pk_bf16_f32 v56, v44, v45\n"
    "v_add_f32 v23, v23, v45\n"
    "v_mfma_f32_16x16x32_bf16 a[96:99], v[104:107], v[52:55], a[96:99]\n"
    "v_exp_f32 v46, v46\n"
    "v_mfma_f32_16x16x32_bf16 a[100:103], v[108:111], v[52:55], a[100:103]\n"
    "v_add_f32 v23, v23, v46\n"
    "s_waitcnt lgkmcnt(0)\n"
    "v_mfma_f32_16x16x32_bf16 a[104:107], v[112:115], v[52:55], a[104:107]\n"
    "v_exp_f32 v47, v47\n"
    "v_mfma_f32_16x16x32_bf16 a[108:111], v[116:119], v[52:55], a[108:111]\n"
    "v_cvt_pk_bf16_f32 v57, v46, v47\n"
    "v_add_f32 v23, v23, v47\n"
    "v_mfma_f32_16x16x32_bf16 a[112:115], v[120:123], v[52:55], a[112:115]\n"
    "v_exp_f32 v48, v48\n"
    "v_mfma_f32_16x16x32_bf16 a[116:119], v[124:127], v[52:55], a[116:119]\n"
    "buffer_load_dwordx4 v19, s[40:43], s64 offen offset:3072 lds\n"
    "v_add_f32 v23, v23, v48\n"
    "v_mfma_f32_16x16x32_bf16 v[36:39], v[64:67], a[32:35], 0\n"
    "v_exp_f32 v49, v49\n"
    "s_mov_b32 m0, s73\n"
    "v_mfma_f32_16x16x32_bf16 v[36:39], v[68:71], a[36:39], v[36:39]\n"
    "v_cvt_pk_bf16_f32 v58, v48, v49\n"
    "v_add_f32 v23, v23, v49\n"
    "v_mfma_f32_16x16x32_bf16 v[36:39], v[72:75], a[40:43], v[36:39]\n"
    "v_exp_f32 v50, v50\n"
    "v_mfma_f32_16x16x32_bf16 v[36:39], v[76:79], a[44:47], v[36:39]\n"
    "buffer_load_dwordx4 v16, s[40:43], s65 offen offset:0 lds\n"
    "v_add_f32 v23, v23, v50\n"
    "v_mfma_f32_16x16x32_bf16 v[40:43], v[80:83], a[32:35], 0\n"
    "v_exp_f32 v51, v51\n"
    "v_mfma_f32_16x16x32_bf16 v[40:43], v[84:87], a[36:39], v[40:43]\n"
    "v_cvt_pk_bf16_f32 v59, v50, v51\n"
    "v_add_f32 v23, v23, v51\n"
    "v_mfma_f32_16x16x32_bf16 v[40:43], v[88:91], a[40:43], v[40:43]\n"
    "v_exp_f32 v36, v36\n"
    "v_mfma_f32_16x16x32_bf16 v[40:43], v[92:95], a[44:47], v[40:43]\n"
    "buffer_load_dwordx4 v17, s[40:43], s65 offen offset:1024 lds\n"
    "v_add_f32 v24, v24, v36\n"
    "v_mfma_f32_16x16x32_bf16 v[136:139], v[96:99], v[56:59], v[136:139]\n"
    "v_exp_f32 v37, v37\n"
    "v_mfma_f32_16x16x32_bf16 v[140:143], v[100:103], v[56:59], v[140:143]\n"
    "v_cvt_pk_bf16_f32 v52, v36, v37\n"
    "v_add_f32 v24, v24, v37\n"
    "v_mfma_f32_16x16x32_bf16 a[120:123], v[104:107], v[56:59], a[120:123]\n"
    "v_exp_f32 v38, v38\n"
    "v_mfma_f32_16x16x32_bf16 a[124:127], v[108:111], v[56:59], a[124:127]\n"
    "buffer_load_dwordx4 v18, s[40:43], s65 offen offset:2048 lds\n"
    "v_add_f32 v24, v24, v38\n"
    "v_mfma_f32_16x16x32_bf16 a[128:131], v[112:115], v[56:59], a[128:131]\n"
    "v_exp_f32 v39, v39\n"
    "v_mfma_f32_16x16x32_bf16 a[132:135], v[116:119], v[56:59], a[132:135]\n"
    "v_cvt_pk_bf16_f32 v53, v38, v39\n"
    "v_add_f32 v24, v24, v39\n"
    "v_mfma_f32_16x16x32_bf16 a[136:139], v[120:123], v[56:59], a[136:139]\n"
    "v_exp_f32 v40, v40\n"
    "v_mfma_f32_16x16x32_bf16 a[140:143], v[124:127], v[56:59], a[140:143]\n"
    "buffer_load_dwordx4 v19, s[40:43], s65 offen offset:3072 lds\n"
    "v_add_f32 v24, v24, v40\n"
    "v_mfma_f32_16x16x32_bf16 v[44:47], v[64:67], a[48:51], 0\n"
    "v_exp_f32 v41, v41\n"
    "s_mov_b32 m0, s74\n"
    "v_mfma_f32_16x16x32_bf16 v[44:47], v[68:71], a[52:55], v[44:47]\n"
    "v_cvt_pk_bf16_f32 v54, v40, v41\n"
    "v_add_f32 v24, v24, v41\n"
    "v_mfma_f32_16x16x32_bf16 v[44:47], v[72:75], a[56:59], v[44:47]\n"
    "v_exp_f32 v42, v42\n"
    "v_mfma_f32_16x16x32_bf16 v[44:47], v[76:79], a[60:63], v[44:47]\n"
    "buffer_load_dwordx4 v20, s[44:47], s64 offen offset:0 lds\n"
    "v_add_f32 v24, v24, v42\n"
    "v_mfma_f32_16x16x32_bf16 v[48:51], v[80:83], a[48:51], 0\n"
    "v_exp_f32 v43, v43\n"
    "v_mfma_f32_16x16x32_bf16 v[48:51], v[84:87], a[52:55], v[48:51]\n"
    "v_cvt_pk_bf16_f32 v55, v42, v43\n"
    "v_add_f32 v24, v24, v43\n"
    "v_mfma_f32_16x16x32_bf16 v[48:51], v[88:91], a[56:59], v[48:51]\n"
    "v_exp_f32 v44, v44\n"
    "v_mfma_f32_16x16x32_bf16 v[48:51], v[92:95], a[60:63], v[48:51]\n"
    "buffer_load_dwordx4 v21, s[44:47], s64 offen offset:1024 lds\n"
    "v_add_f32 v25, v25, v44\n"
    "v_mfma_f32_16x16x32_bf16 v[144:147], v[96:99], v[52:55], v[144:147]\n"
    "v_exp_f32 v45, v45\n"
    "v_mfma_f32_16x16x32_bf16 v[148:151], v[100:103], v[52:55], v[148:151]\n"
    "v_cvt_pk_bf16_f32 v56, v44, v45\n"
    "v_add_f32 v25, v25, v45\n"
    "v_mfma_f32_16x16x32_bf16 a[144:147], v[104:107], v[52:55], a[144:147]\n"
    "v_exp_f32 v46, v46\n"
    "v_mfma_f32_16x16x32_bf16 a[148:151], v[108:111], v[52:55], a[148:151]\n"
    "buffer_load_dwordx4 v20, s[44:47], s64 offen offset:2048 lds\n"
    "v_add_f32 v25, v25, v46\n"
    "v_mfma_f32_16x16x32_bf16 a[152:155], v[112:115], v[52:55], a[152:155]\n"
    "v_exp_f32 v47, v47\n"
    "v_mfma_f32_16x16x32_bf16 a[156:159], v[116:119], v[52:55], a[156:159]\n"
    "v_cvt_pk_bf16_f32 v57, v46, v47\n"
    "v_add_f32 v25, v25, v47\n"
    "v_mfma_f32_16x16x32_bf16 a[160:163], v[120:123], v[52:55], a[160:163]\n"
    "v_exp_f32 v48, v48\n"
    "v_mfma_f32_16x16x32_bf16 a[164:167], v[124:127], v[52:55], a[164:167]\n"
    "buffer_load_dwordx4 v21, s[44:47], s64 offen offset:3072 lds\n"
    "v_add_f32 v25, v25, v48\n"
    "v_mfma_f32_16x16x32_bf16 v[36:39], v[64:67], a[64:67], 0\n"
    "v_exp_f32 v49, v49\n"
    "s_mov_b32 m0, s75\n"
    "v_mfma_f32_16x16x32_bf16 v[36:39], v[68:71], a[68:71], v[36:39]\n"
    "v_cvt_pk_bf16_f32 v58, v48, v49\n"
    "v_add_f32 v25, v25, v49\n"
    "v_mfma_f32_16x16x32_bf16 v[36:39], v[72:75], a[72:75], v[36:39]\n"
    "v_exp_f32 v50, v50\n"
    "v_mfma_f32_16x16x32_bf16 v[36:39], v[76:79], a[76:79], v[36:39]\n"
    "buffer_load_dwordx4 v20, s[44:47], s65 offen offset:0 lds\n"
    "v_add_f32 v25, v25, v50\n"
    "v_mfma_f32_16x16x32_bf16 v[40:43], v[80:83], a[64:67], 0\n"
    "v_exp_f32 v51, v51\n"
    "v_mfma_f32_16x16x32_bf16 v[40:43], v[84:87], a[68:71], v[40:43]\n"
    "v_cvt_pk_bf16_f32 v59, v50, v51\n"
    "v_add_f32 v25, v25, v51\n"
    "v_mfma_f32_16x16x32_bf16 v[40:43], v[88:91], a[72:75], v[40:43]\n"
    "v_exp_f32 v36, v36\n"
    "v_mfma_f32_16x16x32_bf16 v[40:43], v[92:95], a[76:79], v[40:43]\n"
    "buffer_load_dwordx4 v21, s[44:47], s65 offen offset:1024 lds\n"
    "v_add_f32 v26, v26, v36\n"
    "v_mfma_f32_16x16x32_bf16 v[152:155], v[96:99], v[56:59], v[152:155]\n"
    "v_exp_f32 v37, v37\n"
    "v_mfma_f32_16x16x32_bf16 v[156:159], v[100:103], v[56:59], v[156:159]\n"
    "v_cvt_pk_bf16_f32 v52, v36, v37\n"
    "v_add_f32 v26, v26, v37\n"
    "v_mfma_f32_16x16x32_bf16 a[168:171], v[104:107], v[56:59], a[168:171]\n"
    "v_exp_f32 v38, v38\n"
    "v_mfma_f32_16x16x32_bf16 a[172:175], v[108:111], v[56:59], a[172:175]\n"
    "buffer_load_dwordx4 v20, s[44:47], s65 offen offset:2048 lds\n"
    "v_add_f32 v26, v26, v38\n"
    "v_mfma_f32_16x16x32_bf16 a[176:179], v[112:115], v[56:59], a[176:179]\n"
    "v_exp_f32 v39, v39\n"
    "v_mfma_f32_16x16x32_bf16 a[180:183], v[116:119], v[56:59], a[180:183]\n"
    "v_cvt_pk_bf16_f32 v53, v38, v39\n"
    "v_add_f32 v26, v26, v39\n"
    "v_mfma_f32_16x16x32_bf16 a[184:187], v[120:123], v[56:59], a[184:187]\n"
    "v_exp_f32 v40, v40\n"
    "v_mfma_f32_16x16x32_bf16 a[188:191], v[124:127], v[56:59], a[188:191]\n"
    "buffer_load_dwordx4 v21, s[44:47], s65 offen offset:3072 lds\n"
    "v_add_f32 v26, v26, v40\n"
    "v_mfma_f32_16x16x32_bf16 v[44:47], v[64:67], a[80:83], 0\n"
    "s_waitcnt vmcnt(24)\n"
    "ds_read_b128 v[64:67], v4 offset:16384\n"
    "v_exp_f32 v41, v41\n"
    "v_mfma_f32_16x16x32_bf16 v[44:47], v[68:71], a[84:87], v[44:47]\n"
    "ds_read_b128 v[68:71], v5 offset:16384\n"
    "v_cvt_pk_bf16_f32 v54, v40, v41\n"
    "v_add_f32 v26, v26, v41\n"
    "v_mfma_f32_16x16x32_bf16 v[44:47], v[72:75], a[88:91], v[44:47]\n"
    "ds_read_b128 v[72:75], v6 offset:16384\n"
    "v_exp_f32 v42, v42\n"
    "v_mfma_f32_16x16x32_bf16 v[44:47], v[76:79], a[92:95], v[44:47]\n"
    "ds_read_b128 v[76:79], v7 offset:16384\n"
    "v_add_f32 v26, v26, v42\n"
    "v_mfma_f32_16x16x32_bf16 v[48:51], v[80:83], a[80:83], 0\n"
    "ds_read_b128 v[80:83], v4 offset:20480\n"
    "v_exp_f32 v43, v43\n"
    "v_mfma_f32_16x16x32_bf16 v[48:51], v[84:87], a[84:87], v[48:51]\n"
    "ds_read_b128 v[84:87], v5 offset:20480\n"
    "v_cvt_pk_bf16_f32 v55, v42, v43\n"
    "v_add_f32 v26, v26, v43\n"
    "v_mfma_f32_16x16x32_bf16 v[48:51], v[88:91], a[88:91], v[48:51]\n"
    "ds_read_b128 v[88:91], v6 offset:20480\n"
    "v_exp_f32 v44, v44\n"
    "v_mfma_f32_16x16x32_bf16 v[48:51], v[92:95], a[92:95], v[48:51]\n"
    "ds_read_b128 v[92:95], v7 offset:20480\n"
    "v_add_f32 v27, v27, v44\n"
    "v_mfma_f32_16x16x32_bf16 v[160:163], v[96:99], v[52:55], v[160:163]\n"
    "v_exp_f32 v45, v45\n"
    "v_mfma_f32_16x16x32_bf16 v[164:167], v[100:103], v[52:55], v[164:167]\n"
    "v_cvt_pk_bf16_f32 v56, v44, v45\n"
    "v_add_f32 v27, v27, v45\n"
    "v_mfma_f32_16x16x32_bf16 a[192:195], v[104:107], v[52:55], a[192:195]\n"
    "v_exp_f32 v46, v46\n"
    "v_mfma_f32_16x16x32_bf16 a[196:199], v[108:111], v[52:55], a[196:199]\n"
    "v_add_f32 v27, v27, v46\n"
    "v_mfma_f32_16x16x32_bf16 a[200:203], v[112:115], v[52:55], a[200:203]\n"
    "v_exp_f32 v47, v47\n"
    "v_mfma_f32_16x16x32_bf16 a[204:207], v[116:119], v[52:55], a[204:207]\n"
    "v_cvt_pk_bf16_f32 v57, v46, v47\n"
    "v_add_f32 v27, v27, v47\n"
    "v_mfma_f32_16x16x32_bf16 a[208:211], v[120:123], v[52:55], a[208:211]\n"
    "v_exp_f32 v48, v48\n"
    "v_mfma_f32_16x16x32_bf16 a[212:215], v[124:127], v[52:55], a[212:215]\n"
    "v_add_f32 v27, v27, v48\n"
    "s_waitcnt lgkmcnt(4)\n"
    "v_mfma_f32_16x16x32_bf16 v[36:39], v[64:67], a[0:3], 0\n"
    "v_exp_f32 v49, v49\n"
    "v_mfma_f32_16x16x32_bf16 v[36:39], v[68:71], a[4:7], v[36:39]\n"
    "v_cvt_pk_bf16_f32 v58, v48, v49\n"
    "v_add_f32 v27, v27, v49\n"
    "v_mfma_f32_16x16x32_bf16 v[36:39], v[72:75], a[8:11], v[36:39]\n"
    "v_exp_f32 v50, v50\n"
    "v_mfma_f32_16x16x32_bf16 v[36:39], v[76:79], a[12:15], v[36:39]\n"
    "v_add_f32 v27, v27, v50\n"
    "s_waitcnt lgkmcnt(0)\n"
    "v_mfma_f32_16x16x32_bf16 v[40:43], v[80:83], a[0:3], 0\n"
    "v_exp_f32 v51, v51\n"
    "v_mfma_f32_16x16x32_bf16 v[40:43], v[84:87], a[4:7], v[40:43]\n"
    "v_cvt_pk_bf16_f32 v59, v50, v51\n"
    "v_add_f32 v27, v27, v51\n"
    "v_mfma_f32_16x16x32_bf16 v[40:43], v[88:91], a[8:11], v[40:43]\n"
    "v_exp_f32 v36, v36\n"
    "v_mfma_f32_16x16x32_bf16 v[40:43], v[92:95], a[12:15], v[40:43]\n"
    "v_add_f32 v22, v22, v36\n"
    "s_mov_b32 m0, s76\n"
    "v_mfma_f32_16x16x32_bf16 v[168:171], v[96:99], v[56:59], v[168:171]\n"
    "s_waitcnt vmcnt(16)\n"
    "ds_read_b64_tr_b16 v[96:97], v8 offset:24576\n"
    "ds_read_b64_tr_b16 v[98:99], v8 offset:28672\n"
    "v_exp_f32 v37, v37\n"
    "v_mfma_f32_16x16x32_bf16 v[172:175], v[100:103], v[56:59], v[172:175]\n"
    "ds_read_b64_tr_b16 v[100:101], v9 offset:24576\n"
    "ds_read_b64_tr_b16 v[102:103], v9 offset:28672\n"
    "v_cvt_pk_bf16_f32 v52, v36, v37\n"
    "v_add_f32 v22, v22, v37\n"
    "v_mfma_f32_16x16x32_bf16 a[216:219], v[104:107], v[56:59], a[216:219]\n"
    "ds_read_b64_tr_b16 v[104:105], v10 offset:24576\n"
    "ds_read_b64_tr_b16 v[106:107], v10 offset:28672\n"
    "v_exp_f32 v38, v38\n"
    "v_mfma_f32_16x16x32_bf16 a[220:223], v[108:111], v[56:59], a[220:223]\n"
    "ds_read_b64_tr_b16 v[108:109], v11 offset:24576\n"
    "ds_read_b64_tr_b16 v[110:111], v11 offset:28672\n"
    "buffer_load_dwordx4 v16, s[40:43], s66 offen offset:0 lds\n"
    "v_add_f32 v22, v22, v38\n"
    "v_mfma_f32_16x16x32_bf16 a[224:227], v[112:115], v[56:59], a[224:227]\n"
    "ds_read_b64_tr_b16 v[112:113], v12 offset:24576\n"
    "ds_read_b64_tr_b16 v[114:115], v12 offset:28672\n"
    "v_exp_f32 v39, v39\n"
    "v_mfma_f32_16x16x32_bf16 a[228:231], v[116:119], v[56:59], a[228:231]\n"
    "ds_read_b64_tr_b16 v[116:117], v13 offset:24576\n"
    "ds_read_b64_tr_b16 v[118:119], v13 offset:28672\n"
    "v_cvt_pk_bf16_f32 v53, v38, v39\n"
    "v_add_f32 v22, v22, v39\n"
    "v_mfma_f32_16x16x32_bf16 a[232:235], v[120:123], v[56:59], a[232:235]\n"
    "ds_read_b64_tr_b16 v[120:121], v14 offset:24576\n"
    "ds_read_b64_tr_b16 v[122:123], v14 offset:28672\n"
    "v_exp_f32 v40, v40\n"
    "v_mfma_f32_16x16x32_bf16 a[236:239], v[124:127], v[56:59], a[236:239]\n"
    "ds_read_b64_tr_b16 v[124:125], v15 offset:24576\n"
    "ds_read_b64_tr_b16 v[126:127], v15 offset:28672\n"
    "buffer_load_dwordx4 v17, s[40:43], s66 offen offset:1024 lds\n"
    "v_add_f32 v22, v22, v40\n"
    "v_mfma_f32_16x16x32_bf16 v[44:47], v[64:67], a[16:19], 0\n"
    "v_exp_f32 v41, v41\n"
    "v_mfma_f32_16x16x32_bf16 v[44:47], v[68:71], a[20:23], v[44:47]\n"
    "v_cvt_pk_bf16_f32 v54, v40, v41\n"
    "v_add_f32 v22, v22, v41\n"
    "v_mfma_f32_16x16x32_bf16 v[44:47], v[72:75], a[24:27], v[44:47]\n"
    "v_exp_f32 v42, v42\n"
    "v_mfma_f32_16x16x32_bf16 v[44:47], v[76:79], a[28:31], v[44:47]\n"
    "buffer_load_dwordx4 v18, s[40:43], s66 offen offset:2048 lds\n"
    "v_add_f32 v22, v22, v42\n"
    "v_mfma_f32_16x16x32_bf16 v[48:51], v[80:83], a[16:19], 0\n"
    "buffer_load_dwordx4 v19, s[40:43], s66 offen offset:3072 lds\n"
    "v_exp_f32 v43, v43\n"
    "v_mfma_f32_16x16x32_bf16 v[48:51], v[84:87], a[20:23], v[48:51]\n"
    "v_cvt_pk_bf16_f32 v55, v42, v43\n"
    "v_add_f32 v22, v22, v43\n"
    "s_mov_b32 m0, s77\n"
    "v_mfma_f32_16x16x32_bf16 v[48:51], v[88:91], a[24:27], v[48:51]\n"
    "buffer_load_dwordx4 v16, s[40:43], s67 offen offset:0 lds\n"
    "v_exp_f32 v44, v44\n"
    "v_mfma_f32_16x16x32_bf16 v[48:51], v[92:95], a[28:31], v[48:51]\n"
    "v_add_f32 v23, v23, v44\n"
    "s_waitcnt lgkmcnt(8)\n"
    "v_mfma_f32_16x16x32_bf16 v[128:131], v[96:99], v[52:55], v[128:131]\n"
    "v_exp_f32 v45, v45\n"
    "v_mfma_f32_16x16x32_bf16 v[132:135], v[100:103], v[52:55], v[132:135]\n"
    "v_cvt_pk_bf16_f32 v56, v44, v45\n"
    "v_add_f32 v23, v23, v45\n"
    "v_mfma_f32_16x16x32_bf16 a[96:99], v[104:107], v[52:55], a[96:99]\n"
    "v_exp_f32 v46, v46\n"
    "v_mfma_f32_16x16x32_bf16 a[100:103], v[108:111], v[52:55], a[100:103]\n"
    "v_add_f32 v23, v23, v46\n"
    "s_waitcnt lgkmcnt(0)\n"
    "v_mfma_f32_16x16x32_bf16 a[104:107], v[112:115], v[52:55], a[104:107]\n"
    "v_exp_f32 v47, v47\n"
    "v_mfma_f32_16x16x32_bf16 a[108:111], v[116:119], v[52:55], a[108:111]\n"
    "v_cvt_pk_bf16_f32 v57, v46, v47\n"
    "v_add_f32 v23, v23, v47\n"
    "v_mfma_f32_16x16x32_bf16 a[112:115], v[120:123], v[52:55], a[112:115]\n"
    "v_exp_f32 v48, v48\n"
    "v_mfma_f32_16x16x32_bf16 a[116:119], v[124:127], v[52:55], a[116:119]\n"
    "buffer_load_dwordx4 v17, s[40:43], s67 offen offset:1024 lds\n"
    "v_add_f32 v23, v23, v48\n"
    "v_mfma_f32_16x16x32_bf16 v[36:39], v[64:67], a[32:35], 0\n"
    "v_exp_f32 v49, v49\n"
    "v_mfma_f32_16x16x32_bf16 v[36:39], v[68:71], a[36:39], v[36:39]\n"
    "v_cvt_pk_bf16_f32 v58, v48, v49\n"
    "v_add_f32 v23, v23, v49\n"
    "v_mfma_f32_16x16x32_bf16 v[36:39], v[72:75], a[40:43], v[36:39]\n"
    "v_exp_f32 v50, v50\n"
    "v_mfma_f32_16x16x32_bf16 v[36:39], v[76:79], a[44:47], v[36:39]\n"
    "buffer_load_dwordx4 v18, s[40:43], s67 offen offset:2048 lds\n"
    "v_add_f32 v23, v23, v50\n"
    "v_mfma_f32_16x16x32_bf16 v[40:43], v[80:83], a[32:35], 0\n"
    "v_exp_f32 v51, v51\n"
    "v_mfma_f32_16x16x32_bf16 v[40:43], v[84:87], a[36:39], v[40:43]\n"
    "v_cvt_pk_bf16_f32 v59, v50, v51\n"
    "v_add_f32 v23, v23, v51\n"
    "v_mfma_f32_16x16x32_bf16 v[40:43], v[88:91], a[40:43], v[40:43]\n"
    "v_exp_f32 v36, v36\n"
    "v_mfma_f32_16x16x32_bf16 v[40:43], v[92:95], a[44:47], v[40:43]\n"
    "buffer_load_dwordx4 v19, s[40:43], s67 offen offset:3072 lds\n"
    "v_add_f32 v24, v24, v36\n"
    "v_mfma_f32_16x16x32_bf16 v[136:139], v[96:99], v[56:59], v[136:139]\n"
    "v_exp_f32 v37, v37\n"
    "s_mov_b32 m0, s78\n"
    "v_mfma_f32_16x16x32_bf16 v[140:143], v[100:103], v[56:59], v[140:143]\n"
    "v_cvt_pk_bf16_f32 v52, v36, v37\n"
    "v_add_f32 v24, v24, v37\n"
    "v_mfma_f32_16x16x32_bf16 a[120:123], v[104:107], v[56:59], a[120:123]\n"
    "v_exp_f32 v38, v38\n"
    "v_mfma_f32_16x16x32_bf16 a[124:127], v[108:111], v[56:59], a[124:127]\n"
    "buffer_load_dwordx4 v20, s[44:47], s66 offen offset:0 lds\n"
    "v_add_f32 v24, v24, v38\n"
    "v_mfma_f32_16x16x32_bf16 a[128:131], v[112:115], v[56:59], a[128:131]\n"
    "v_exp_f32 v39, v39\n"
    "v_mfma_f32_16x16x32_bf16 a[132:135], v[116:119], v[56:59], a[132:135]\n"
    "v_cvt_pk_bf16_f32 v53, v38, v39\n"
    "v_add_f32 v24, v24, v39\n"
    "v_mfma_f32_16x16x32_bf16 a[136:139], v[120:123], v[56:59], a[136:139]\n"
    "v_exp_f32 v40, v40\n"
    "v_mfma_f32_16x16x32_bf16 a[140:143], v[124:127], v[56:59], a[140:143]\n"
    "buffer_load_dwordx4 v21, s[44:47], s66 offen offset:1024 lds\n"
    "v_add_f32 v24, v24, v40\n"
    "v_mfma_f32_16x16x32_bf16 v[44:47], v[64:67], a[48:51], 0\n"
    "v_exp_f32 v41, v41\n"
    "v_mfma_f32_16x16x32_bf16 v[44:47], v[68:71], a[52:55], v[44:47]\n"
    "v_cvt_pk_bf16_f32 v54, v40, v41\n"
    "v_add_f32 v24, v24, v41\n"
    "v_mfma_f32_16x16x32_bf16 v[44:47], v[72:75], a[56:59], v[44:47]\n"
    "v_exp_f32 v42, v42\n"
    "v_mfma_f32_16x16x32_bf16 v[44:47], v[76:79], a[60:63], v[44:47]\n"
    "buffer_load_dwordx4 v20, s[44:47], s66 offen offset:2048 lds\n"
    "v_add_f32 v24, v24, v42\n"
    "v_mfma_f32_16x16x32_bf16 v[48:51], v[80:83], a[48:51], 0\n"
    "v_exp_f32 v43, v43\n"
    "v_mfma_f32_16x16x32_bf16 v[48:51], v[84:87], a[52:55], v[48:51]\n"
    "v_cvt_pk_bf16_f32 v55, v42, v43\n"
    "v_add_f32 v24, v24, v43\n"
    "v_mfma_f32_16x16x32_bf16 v[48:51], v[88:91], a[56:59], v[48:51]\n"
    "v_exp_f32 v44, v44\n"
    "v_mfma_f32_16x16x32_bf16 v[48:51], v[92:95], a[60:63], v[48:51]\n"
    "buffer_load_dwordx4 v21, s[44:47], s66 offen offset:3072 lds\n"
    "v_add_f32 v25, v25, v44\n"
    "v_mfma_f32_16x16x32_bf16 v[144:147], v[96:99], v[52:55], v[144:147]\n"
    "v_exp_f32 v45, v45\n"
    "s_mov_b32 m0, s79\n"
    "v_mfma_f32_16x16x32_bf16 v[148:151], v[100:103], v[52:55], v[148:151]\n"
    "v_cvt_pk_bf16_f32 v56, v44, v45\n"
    "v_add_f32 v25, v25, v45\n"
    "v_mfma_f32_16x16x32_bf16 a[144:147], v[104:107], v[52:55], a[144:147]\n"
    "v_exp_f32 v46, v46\n"
    "v_mfma_f32_16x16x32_bf16 a[148:151], v[108:111], v[52:55], a[148:151]\n"
    "buffer_load_dwordx4 v20, s[44:47], s67 offen offset:0 lds\n"
    "v_add_f32 v25, v25, v46\n"
    "v_mfma_f32_16x16x32_bf16 a[152:155], v[112:115], v[52:55], a[152:155]\n"
    "v_exp_f32 v47, v47\n"
    "v_mfma_f32_16x16x32_bf16 a[156:159], v[116:119], v[52:55], a[156:159]\n"
    "v_cvt_pk_bf16_f32 v57, v46, v47\n"
    "v_add_f32 v25, v25, v47\n"
    "v_mfma_f32_16x16x32_bf16 a[160:163], v[120:123], v[52:55], a[160:163]\n"
    "v_exp_f32 v48, v48\n"
    "v_mfma_f32_16x16x32_bf16 a[164:167], v[124:127], v[52:55], a[164:167]\n"
    "buffer_load_dwordx4 v21, s[44:47], s67 offen offset:1024 lds\n"
    "v_add_f32 v25, v25, v48\n"
    "v_mfma_f32_16x16x32_bf16 v[36:39], v[64:67], a[64:67], 0\n"
    "v_exp_f32 v49, v49\n"
    "v_mfma_f32_16x16x32_bf16 v[36:39], v[68:71], a[68:71], v[36:39]\n"
    "v_cvt_pk_bf16_f32 v58, v48, v49\n"
    "v_add_f32 v25, v25, v49\n"
    "v_mfma_f32_16x16x32_bf16 v[36:39], v[72:75], a[72:75], v[36:39]\n"
    "v_exp_f32 v50, v50\n"
    "v_mfma_f32_16x16x32_bf16 v[36:39], v[76:79], a[76:79], v[36:39]\n"
    "buffer_load_dwordx4 v20, s[44:47], s67 offen offset:2048 lds\n"
    "v_add_f32 v25, v25, v50\n"
    "v_mfma_f32_16x16x32_bf16 v[40:43], v[80:83], a[64:67], 0\n"
    "v_exp_f32 v51, v51\n"
    "v_mfma_f32_16x16x32_bf16 v[40:43], v[84:87], a[68:71], v[40:43]\n"
    "v_cvt_pk_bf16_f32 v59, v50, v51\n"
    "v_add_f32 v25, v25, v51\n"
    "v_mfma_f32_16x16x32_bf16 v[40:43], v[88:91], a[72:75], v[40:43]\n"
    "v_exp_f32 v36, v36\n"
    "v_mfma_f32_16x16x32_bf16 v[40:43], v[92:95], a[76:79], v[40:43]\n"
    "buffer_load_dwordx4 v21, s[44:47], s67 offen offset:3072 lds\n"
    "v_add_f32 v26, v26, v36\n"
    "v_mfma_f32_16x16x32_bf16 v[152:155], v[96:99], v[56:59], v[152:155]\n"
    "v_exp_f32 v37, v37\n"
    "v_mfma_f32_16x16x32_bf16 v[156:159], v[100:103], v[56:59], v[156:159]\n"
    "v_cvt_pk_bf16_f32 v52, v36, v37\n"
    "v_add_f32 v26, v26, v37\n"
    "v_mfma_f32_16x16x32_bf16 a[168:171], v[104:107], v[56:59], a[168:171]\n"
    "v_exp_f32 v38, v38\n"
    "v_mfma_f32_16x16x32_bf16 a[172:175], v[108:111], v[56:59], a[172:175]\n"
    "v_add_f32 v26, v26, v38\n"
    "v_mfma_f32_16x16x32_bf16 a[176:179], v[112:115], v[56:59], a[176:179]\n"
    "v_exp_f32 v39, v39\n"
    "v_mfma_f32_16x16x32_bf16 a[180:183], v[116:119], v[56:59], a[180:183]\n"
    "v_cvt_pk_bf16_f32 v53, v38, v39\n"
    "v_add_f32 v26, v26, v39\n"
    "v_mfma_f32_16x16x32_bf16 a[184:187], v[120:123], v[56:59], a[184:187]\n"
    "v_exp_f32 v40, v40\n"
    "v_mfma_f32_16x16x32_bf16 a[188:191], v[124:127], v[56:59], a[188:191]\n"
    "v_add_f32 v26, v26, v40\n"
    "v_mfma_f32_16x16x32_bf16 v[44:47], v[64:67], a[80:83], 0\n"
    "s_waitcnt vmcnt(24)\n"
    "ds_read_b128 v[64:67], v4 offset:0\n"
    "v_exp_f32 v41, v41\n"
    "v_mfma_f32_16x16x32_bf16 v[44:47], v[68:71], a[84:87], v[44:47]\n"
    "ds_read_b128 v[68:71], v5 offset:0\n"
    "v_cvt_pk_bf16_f32 v54, v40, v41\n"
    "v_add_f32 v26, v26, v41\n"
    "v_mfma_f32_16x16x32_bf16 v[44:47], v[72:75], a[88:91], v[44:47]\n"
    "ds_read_b128 v[72:75], v6 offset:0\n"
    "v_exp_f32 v42, v42\n"
    "v_mfma_f32_16x16x32_bf16 v[44:47], v[76:79], a[92:95], v[44:47]\n"
    "ds_read_b128 v[76:79], v7 offset:0\n"
    "v_add_f32 v26, v26, v42\n"
    "v_mfma_f32_16x16x32_bf16 v[48:51], v[80:83], a[80:83], 0\n"
    "ds_read_b128 v[80:83], v4 offset:4096\n"
    "v_exp_f32 v43, v43\n"
    "v_mfma_f32_16x16x32_bf16 v[48:51], v[84:87], a[84:87], v[48:51]\n"
    "ds_read_b128 v[84:87], v5 offset:4096\n"
    "v_cvt_pk_bf16_f32 v55, v42, v43\n"
    "v_add_f32 v26, v26, v43\n"
    "v_mfma_f32_16x16x32_bf16 v[48:51], v[88:91], a[88:91], v[48:51]\n"
    "ds_read_b128 v[88:91], v6 offset:4096\n"
    "v_exp_f32 v44, v44\n"
    "v_mfma_f32_16x16x32_bf16 v[48:51], v[92:95], a[92:95], v[48:51]\n"
    "ds_read_b128 v[92:95], v7 offset:4096\n"
    "v_add_f32 v27, v27, v44\n"
    "v_mfma_f32_16x16x32_bf16 v[160:163], v[96:99], v[52:55], v[160:163]\n"
    "v_exp_f32 v45, v45\n"
    "v_mfma_f32_16x16x32_bf16 v[164:167], v[100:103], v[52:55], v[164:167]\n"
    "v_cvt_pk_bf16_f32 v56, v44, v45\n"
    "v_add_f32 v27, v27, v45\n"
    "v_mfma_f32_16x16x32_bf16 a[192:195], v[104:107], v[52:55], a[192:195]\n"
    "v_exp_f32 v46, v46\n"
    "v_mfma_f32_16x16x32_bf16 a[196:199], v[108:111], v[52:55], a[196:199]\n"
    "v_add_f32 v27, v27, v46\n"
    "v_mfma_f32_16x16x32_bf16 a[200:203], v[112:115], v[52:55], a[200:203]\n"
    "v_exp_f32 v47, v47\n"
    "v_mfma_f32_16x16x32_bf16 a[204:207], v[116:119], v[52:55], a[204:207]\n"
    "v_cvt_pk_bf16_f32 v57, v46, v47\n"
    "v_add_f32 v27, v27, v47\n"
    "v_mfma_f32_16x16x32_bf16 a[208:211], v[120:123], v[52:55], a[208:211]\n"
    "v_exp_f32 v48, v48\n"
    "v_mfma_f32_16x16x32_bf16 a[212:215], v[124:127], v[52:55], a[212:215]\n"
    "v_add_f32 v27, v27, v48\n"
    "s_waitcnt lgkmcnt(4)\n"
    "v_mfma_f32_16x16x32_bf16 v[36:39], v[64:67], a[0:3], 0\n"
    "v_exp_f32 v49, v49\n"
    "v_mfma_f32_16x16x32_bf16 v[36:39], v[68:71], a[4:7], v[36:39]\n"
    "v_cvt_pk_bf16_f32 v58, v48, v49\n"
    "v_add_f32 v27, v27, v49\n"
    "v_mfma_f32_16x16x32_bf16 v[36:39], v[72:75], a[8:11], v[36:39]\n"
    "v_exp_f32 v50, v50\n"
    "v_mfma_f32_16x16x32_bf16 v[36:39], v[76:79], a[12:15], v[36:39]\n"
    "v_add_f32 v27, v27, v50\n"
    "s_waitcnt lgkmcnt(0)\n"
    "v_mfma_f32_16x16x32_bf16 v[40:43], v[80:83], a[0:3], 0\n"
    "v_exp_f32 v51, v51\n"
    "v_mfma_f32_16x16x32_bf16 v[40:43], v[84:87], a[4:7], v[40:43]\n"
    "v_cvt_pk_bf16_f32 v59, v50, v51\n"
    "v_add_f32 v27, v27, v51\n"
    "v_mfma_f32_16x16x32_bf16 v[40:43], v[88:91], a[8:11], v[40:43]\n"
    "v_exp_f32 v36, v36\n"
    "v_mfma_f32_16x16x32_bf16 v[40:43], v[92:95], a[12:15], v[40:43]\n"
    "v_add_f32 v22, v22, v36\n"
    "s_mov_b32 m0, s72\n"
    "v_mfma_f32_16x16x32_bf16 v[168:171], v[96:99], v[56:59], v[168:171]\n"
    "s_waitcnt vmcnt(16)\n"
    "ds_read_b64_tr_b16 v[96:97], v8 offset:8192\n"
    "ds_read_b64_tr_b16 v[98:99], v8 offset:12288\n"
    "v_exp_f32 v37, v37\n"
    "v_mfma_f32_16x16x32_bf16 v[172:175], v[100:103], v[56:59], v[172:175]\n"
    "ds_read_b64_tr_b16 v[100:101], v9 offset:8192\n"
    "ds_read_b64_tr_b16 v[102:103], v9 offset:12288\n"
    "v_cvt_pk_bf16_f32 v52, v36, v37\n"
    "v_add_f32 v22, v22, v37\n"
    "v_mfma_f32_16x16x32_bf16 a[216:219], v[104:107], v[56:59], a[216:219]\n"
    "ds_read_b64_tr_b16 v[104:105], v10 offset:8192\n"
    "ds_read_b64_tr_b16 v[106:107], v10 offset:12288\n"
    "v_exp_f32 v38, v38\n"
    "v_mfma_f32_16x16x32_bf16 a[220:223], v[108:111], v[56:59], a[220:223]\n"
    "ds_read_b64_tr_b16 v[108:109], v11 offset:8192\n"
    "ds_read_b64_tr_b16 v[110:111], v11 offset:12288\n"
    "buffer_load_dwordx4 v16, s[40:43], s68 offen offset:0 lds\n"
    "v_add_f32 v22, v22, v38\n"
    "v_mfma_f32_16x16x32_bf16 a[224:227], v[112:115], v[56:59], a[224:227]\n"
    "ds_read_b64_tr_b16 v[112:113], v12 offset:8192\n"
    "ds_read_b64_tr_b16 v[114:115], v12 offset:12288\n"
    "v_exp_f32 v39, v39\n"
    "v_mfma_f32_16x16x32_bf16 a[228:231], v[116:119], v[56:59], a[228:231]\n"
    "ds_read_b64_tr_b16 v[116:117], v13 offset:8192\n"
    "ds_read_b64_tr_b16 v[118:119], v13 offset:12288\n"
    "v_cvt_pk_bf16_f32 v53, v38, v39\n"
    "v_add_f32 v22, v22, v39\n"
    "v_mfma_f32_16x16x32_bf16 a[232:235], v[120:123], v[56:59], a[232:235]\n"
    "ds_read_b64_tr_b16 v[120:121], v14 offset:8192\n"
    "ds_read_b64_tr_b16 v[122:123], v14 offset:12288\n"
    "v_exp_f32 v40, v40\n"
    "v_mfma_f32_16x16x32_bf16 a[236:239], v[124:127], v[56:59], a[236:239]\n"
    "ds_read_b64_tr_b16 v[124:125], v15 offset:8192\n"
    "ds_read_b64_tr_b16 v[126:127], v15 offset:12288\n"
    "buffer_load_dwordx4 v17, s[40:43], s68 offen offset:1024 lds\n"
    "v_add_f32 v22, v22, v40\n"
    "v_mfma_f32_16x16x32_bf16 v[44:47], v[64:67], a[16:19], 0\n"
    "v_exp_f32 v41, v41\n"
    "v_mfma_f32_16x16x32_bf16 v[44:47], v[68:71], a[20:23], v[44:47]\n"
    "v_cvt_pk_bf16_f32 v54, v40, v41\n"
    "v_add_f32 v22, v22, v41\n"
    "v_mfma_f32_16x16x32_bf16 v[44:47], v[72:75], a[24:27], v[44:47]\n"
    "v_exp_f32 v42, v42\n"
    "v_mfma_f32_16x16x32_bf16 v[44:47], v[76:79], a[28:31], v[44:47]\n"
    "buffer_load_dwordx4 v18, s[40:43], s68 offen offset:2048 lds\n"
    "v_add_f32 v22, v22, v42\n"
    "v_mfma_f32_16x16x32_bf16 v[48:51], v[80:83], a[16:19], 0\n"
    "buffer_load_dwordx4 v19, s[40:43], s68 offen offset:3072 lds\n"
    "v_exp_f32 v43, v43\n"
    "v_mfma_f32_16x16x32_bf16 v[48:51], v[84:87], a[20:23], v[48:51]\n"
    "v_cvt_pk_bf16_f32 v55, v42, v43\n"
    "v_add_f32 v22, v22, v43\n"
    "s_mov_b32 m0, s73\n"
    "v_mfma_f32_16x16x32_bf16 v[48:51], v[88:91], a[24:27], v[48:51]\n"
    "buffer_load_dwordx4 v16, s[40:43], s69 offen offset:0 lds\n"
    "v_exp_f32 v44, v44\n"
    "v_mfma_f32_16x16x32_bf16 v[48:51], v[92:95], a[28:31], v[48:51]\n"
    "v_add_f32 v23, v23, v44\n"
    "s_waitcnt lgkmcnt(8)\n"
    "v_mfma_f32_16x16x32_bf16 v[128:131], v[96:99], v[52:55], v[128:131]\n"
    "v_exp_f32 v45, v45\n"
    "v_mfma_f32_16x16x32_bf16 v[132:135], v[100:103], v[52:55], v[132:135]\n"
    "v_cvt_pk_bf16_f32 v56, v44, v45\n"
    "v_add_f32 v23, v23, v45\n"
    "v_mfma_f32_16x16x32_bf16 a[96:99], v[104:107], v[52:55], a[96:99]\n"
    "v_exp_f32 v46, v46\n"
    "v_mfma_f32_16x16x32_bf16 a[100:103], v[108:111], v[52:55], a[100:103]\n"
    "v_add_f32 v23, v23, v46\n"
    "s_waitcnt lgkmcnt(0)\n"
    "v_mfma_f32_16x16x32_bf16 a[104:107], v[112:115], v[52:55], a[104:107]\n"
    "v_exp_f32 v47, v47\n"
    "v_mfma_f32_16x16x32_bf16 a[108:111], v[116:119], v[52:55], a[108:111]\n"
    "v_cvt_pk_bf16_f32 v57, v46, v47\n"
    "v_add_f32 v23, v23, v47\n"
    "v_mfma_f32_16x16x32_bf16 a[112:115], v[120:123], v[52:55], a[112:115]\n"
    "v_exp_f32 v48, v48\n"
    "v_mfma_f32_16x16x32_bf16 a[116:119], v[124:127], v[52:55], a[116:119]\n"
    "buffer_load_dwordx4 v17, s[40:43], s69 offen offset:1024 lds\n"
    "v_add_f32 v23, v23, v48\n"
    "v_mfma_f32_16x16x32_bf16 v[36:39], v[64:67], a[32:35], 0\n"
    "v_exp_f32 v49, v49\n"
    "v_mfma_f32_16x16x32_bf16 v[36:39], v[68:71], a[36:39], v[36:39]\n"
    "v_cvt_pk_bf16_f32 v58, v48, v49\n"
    "v_add_f32 v23, v23, v49\n"
    "v_mfma_f32_16x16x32_bf16 v[36:39], v[72:75], a[40:43], v[36:39]\n"
    "v_exp_f32 v50, v50\n"
    "v_mfma_f32_16x16x32_bf16 v[36:39], v[76:79], a[44:47], v[36:39]\n"
    "buffer_load_dwordx4 v18, s[40:43], s69 offen offset:2048 lds\n"
    "v_add_f32 v23, v23, v50\n"
    "v_mfma_f32_16x16x32_bf16 v[40:43], v[80:83], a[32:35], 0\n"
    "v_exp_f32 v51, v51\n"
    "v_mfma_f32_16x16x32_bf16 v[40:43], v[84:87], a[36:39], v[40:43]\n"
    "v_cvt_pk_bf16_f32 v59, v50, v51\n"
    "v_add_f32 v23, v23, v51\n"
    "v_mfma_f32_16x16x32_bf16 v[40:43], v[88:91], a[40:43], v[40:43]\n"
    "v_exp_f32 v36, v36\n"
    "v_mfma_f32_16x16x32_bf16 v[40:43], v[92:95], a[44:47], v[40:43]\n"
    "buffer_load_dwordx4 v19, s[40:43], s69 offen offset:3072 lds\n"
    "v_add_f32 v24, v24, v36\n"
    "v_mfma_f32_16x16x32_bf16 v[136:139], v[96:99], v[56:59], v[136:139]\n"
    "v_exp_f32 v37, v37\n"
    "s_mov_b32 m0, s74\n"
    "v_mfma_f32_16x16x32_bf16 v[140:143], v[100:103], v[56:59], v[140:143]\n"
    "v_cvt_pk_bf16_f32 v52, v36, v37\n"
    "v_add_f32 v24, v24, v37\n"
    "v_mfma_f32_16x16x32_bf16 a[120:123], v[104:107], v[56:59], a[120:123]\n"
    "v_exp_f32 v38, v38\n"
    "v_mfma_f32_16x16x32_bf16 a[124:127], v[108:111], v[56:59], a[124:127]\n"
    "buffer_load_dwordx4 v20, s[44:47], s68 offen offset:0 lds\n"
    "v_add_f32 v24, v24, v38\n"
    "v_mfma_f32_16x16x32_bf16 a[128:131], v[112:115], v[56:59], a[128:131]\n"
    "v_exp_f32 v39, v39\n"
    "v_mfma_f32_16x16x32_bf16 a[132:135], v[116:119], v[56:59], a[132:135]\n"
    "v_cvt_pk_bf16_f32 v53, v38, v39\n"
    "v_add_f32 v24, v24, v39\n"
    "v_mfma_f32_16x16x32_bf16 a[136:139], v[120:123], v[56:59], a[136:139]\n"
    "v_exp_f32 v40, v40\n"
    "v_mfma_f32_16x16x32_bf16 a[140:143], v[124:127], v[56:59], a[140:143]\n"
    "buffer_load_dwordx4 v21, s[44:47], s68 offen offset:1024 lds\n"
    "v_add_f32 v24, v24, v40\n"
    "v_mfma_f32_16x16x32_bf16 v[44:47], v[64:67], a[48:51], 0\n"
    "v_exp_f32 v41, v41\n"
    "v_mfma_f32_16x16x32_bf16 v[44:47], v[68:71], a[52:55], v[44:47]\n"
    "v_cvt_pk_bf16_f32 v54, v40, v41\n"
    "v_add_f32 v24, v24, v41\n"
    "v_mfma_f32_16x16x32_bf16 v[44:47], v[72:75], a[56:59], v[44:47]\n"
    "v_exp_f32 v42, v42\n"
    "v_mfma_f32_16x16x32_bf16 v[44:47], v[76:79], a[60:63], v[44:47]\n"
    "buffer_load_dwordx4 v20, s[44:47], s68 offen offset:2048 lds\n"
    "v_add_f32 v24, v24, v42\n"
    "v_mfma_f32_16x16x32_bf16 v[48:51], v[80:83], a[48:51], 0\n"
    "v_exp_f32 v43, v43\n"
    "v_mfma_f32_16x16x32_bf16 v[48:51], v[84:87], a[52:55], v[48:51]\n"
    "v_cvt_pk_bf16_f32 v55, v42, v43\n"
    "v_add_f32 v24, v24, v43\n"
    "v_mfma_f32_16x16x32_bf16 v[48:51], v[88:91], a[56:59], v[48:51]\n"
    "v_exp_f32 v44, v44\n"
    "v_mfma_f32_16x16x32_bf16 v[48:51], v[92:95], a[60:63], v[48:51]\n"
    "buffer_load_dwordx4 v21, s[44:47], s68 offen offset:3072 lds\n"
    "v_add_f32 v25, v25, v44\n"
    "v_mfma_f32_16x16x32_bf16 v[144:147], v[96:99], v[52:55], v[144:147]\n"
    "v_exp_f32 v45, v45\n"
    "s_mov_b32 m0, s75\n"
    "v_mfma_f32_16x16x32_bf16 v[148:151], v[100:103], v[52:55], v[148:151]\n"
    "v_cvt_pk_bf16_f32 v56, v44, v45\n"
    "v_add_f32 v25, v25, v45\n"
    "v_mfma_f32_16x16x32_bf16 a[144:147], v[104:107], v[52:55], a[144:147]\n"
    "v_exp_f32 v46, v46\n"
    "v_mfma_f32_16x16x32_bf16 a[148:151], v[108:111], v[52:55], a[148:151]\n"
    "buffer_load_dwordx4 v20, s[44:47], s69 offen offset:0 lds\n"
    "v_add_f32 v25, v25, v46\n"
    "v_mfma_f32_16x16x32_bf16 a[152:155], v[112:115], v[52:55], a[152:155]\n"
    "v_exp_f32 v47, v47\n"
    "v_mfma_f32_16x16x32_bf16 a[156:159], v[116:119], v[52:55], a[156:159]\n"
    "v_cvt_pk_bf16_f32 v57, v46, v47\n"
    "v_add_f32 v25, v25, v47\n"
    "v_mfma_f32_16x16x32_bf16 a[160:163], v[120:123], v[52:55], a[160:163]\n"
    "v_exp_f32 v48, v48\n"
    "v_mfma_f32_16x16x32_bf16 a[164:167], v[124:127], v[52:55], a[164:167]\n"
    "buffer_load_dwordx4 v21, s[44:47], s69 offen offset:1024 lds\n"
    "v_add_f32 v25, v25, v48\n"
    "v_mfma_f32_16x16x32_bf16 v[36:39], v[64:67], a[64:67], 0\n"
    "v_exp_f32 v49, v49\n"
    "v_mfma_f32_16x16x32_bf16 v[36:39], v[68:71], a[68:71], v[36:39]\n"
    "v_cvt_pk_bf16_f32 v58, v48, v49\n"
    "v_add_f32 v25, v25, v49\n"
    "v_mfma_f32_16x16x32_bf16 v[36:39], v[72:75], a[72:75], v[36:39]\n"
    "v_exp_f32 v50, v50\n"
    "v_mfma_f32_16x16x32_bf16 v[36:39], v[76:79], a[76:79], v[36:39]\n"
    "buffer_load_dwordx4 v20, s[44:47], s69 offen offset:2048 lds\n"
    "v_add_f32 v25, v25, v50\n"
    "v_mfma_f32_16x16x32_bf16 v[40:43], v[80:83], a[64:67], 0\n"
    "v_exp_f32 v51, v51\n"
    "v_mfma_f32_16x16x32_bf16 v[40:43], v[84:87], a[68:71], v[40:43]\n"
    "v_cvt_pk_bf16_f32 v59, v50, v51\n"
    "v_add_f32 v25, v25, v51\n"
    "v_mfma_f32_16x16x32_bf16 v[40:43], v[88:91], a[72:75], v[40:43]\n"
    "v_exp_f32 v36, v36\n"
    "v_mfma_f32_16x16x32_bf16 v[40:43], v[92:95], a[76:79], v[40:43]\n"
    "buffer_load_dwordx4 v21, s[44:47], s69 offen offset:3072 lds\n"
    "v_add_f32 v26, v26, v36\n"
    "v_mfma_f32_16x16x32_bf16 v[152:155], v[96:99], v[56:59], v[152:155]\n"
    "v_exp_f32 v37, v37\n"
    "v_mfma_f32_16x16x32_bf16 v[156:159], v[100:103], v[56:59], v[156:159]\n"
    "v_cvt_pk_bf16_f32 v52, v36, v37\n"
    "v_add_f32 v26, v26, v37\n"
    "v_mfma_f32_16x16x32_bf16 a[168:171], v[104:107], v[56:59], a[168:171]\n"
    "v_exp_f32 v38, v38\n"
    "v_mfma_f32_16x16x32_bf16 a[172:175], v[108:111], v[56:59], a[172:175]\n"
    "v_add_f32 v26, v26, v38\n"
    "v_mfma_f32_16x16x32_bf16 a[176:179], v[112:115], v[56:59], a[176:179]\n"
    "v_exp_f32 v39, v39\n"
    "v_mfma_f32_16x16x32_bf16 a[180:183], v[116:119], v[56:59], a[180:183]\n"
    "v_cvt_pk_bf16_f32 v53, v38, v39\n"
    "v_add_f32 v26, v26, v39\n"
    "v_mfma_f32_16x16x32_bf16 a[184:187], v[120:123], v[56:59], a[184:187]\n"
    "v_exp_f32 v40, v40\n"
    "v_mfma_f32_16x16x32_bf16 a[188:191], v[124:127], v[56:59], a[188:191]\n"
    "v_add_f32 v26, v26, v40\n"
    "v_mfma_f32_16x16x32_bf16 v[44:47], v[64:67], a[80:83], 0\n"
    "s_waitcnt vmcnt(24)\n"
    "ds_read_b128 v[64:67], v4 offset:16384\n"
    "v_exp_f32 v41, v41\n"
    "v_mfma_f32_16x16x32_bf16 v[44:47], v[68:71], a[84:87], v[44:47]\n"
    "ds_read_b128 v[68:71], v5 offset:16384\n"
    "v_cvt_pk_bf16_f32 v54, v40, v41\n"
    "v_add_f32 v26, v26, v41\n"
    "v_mfma_f32_16x16x32_bf16 v[44:47], v[72:75], a[88:91], v[44:47]\n"
    "ds_read_b128 v[72:75], v6 offset:16384\n"
    "v_exp_f32 v42, v42\n"
    "v_mfma_f32_16x16x32_bf16 v[44:47], v[76:79], a[92:95], v[44:47]\n"
    "ds_read_b128 v[76:79], v7 offset:16384\n"
    "v_add_f32 v26, v26, v42\n"
    "v_mfma_f32_16x16x32_bf16 v[48:51], v[80:83], a[80:83], 0\n"
    "ds_read_b128 v[80:83], v4 offset:20480\n"
    "v_exp_f32 v43, v43\n"
    "v_mfma_f32_16x16x32_bf16 v[48:51], v[84:87], a[84:87], v[48:51]\n"
    "ds_read_b128 v[84:87], v5 offset:20480\n"
    "v_cvt_pk_bf16_f32 v55, v42, v43\n"
    "v_add_f32 v26, v26, v43\n"
    "v_mfma_f32_16x16x32_bf16 v[48:51], v[88:91], a[88:91], v[48:51]\n"
    "ds_read_b128 v[88:91], v6 offset:20480\n"
    "v_exp_f32 v44, v44\n"
    "v_mfma_f32_16x16x32_bf16 v[48:51], v[92:95], a[92:95], v[48:51]\n"
    "ds_read_b128 v[92:95], v7 offset:20480\n"
    "v_add_f32 v27, v27, v44\n"
    "v_mfma_f32_16x16x32_bf16 v[160:163], v[96:99], v[52:55], v[160:163]\n"
    "v_exp_f32 v45, v45\n"
    "v_mfma_f32_16x16x32_bf16 v[164:167], v[100:103], v[52:55], v[164:167]\n"
    "v_cvt_pk_bf16_f32 v56, v44, v45\n"
    "v_add_f32 v27, v27, v45\n"
    "v_mfma_f32_16x16x32_bf16 a[192:195], v[104:107], v[52:55], a[192:195]\n"
    "v_exp_f32 v46, v46\n"
    "v_mfma_f32_16x16x32_bf16 a[196:199], v[108:111], v[52:55], a[196:199]\n"
    "v_add_f32 v27, v27, v46\n"
    "v_mfma_f32_16x16x32_bf16 a[200:203], v[112:115], v[52:55], a[200:203]\n"
    "v_exp_f32 v47, v47\n"
    "v_mfma_f32_16x16x32_bf16 a[204:207], v[116:119], v[52:55], a[204:207]\n"
    "v_cvt_pk_bf16_f32 v57, v46, v47\n"
    "v_add_f32 v27, v27, v47\n"
    "v_mfma_f32_16x16x32_bf16 a[208:211], v[120:123], v[52:55], a[208:211]\n"
    "v_exp_f32 v48, v48\n"
    "v_mfma_f32_16x16x32_bf16 a[212:215], v[124:127], v[52:55], a[212:215]\n"
    "v_add_f32 v27, v27, v48\n"
    "s_waitcnt lgkmcnt(4)\n"
    "v_mfma_f32_16x16x32_bf16 v[36:39], v[64:67], a[0:3], 0\n"
    "v_exp_f32 v49, v49\n"
    "v_mfma_f32_16x16x32_bf16 v[36:39], v[68:71], a[4:7], v[36:39]\n"
    "v_cvt_pk_bf16_f32 v58, v48, v49\n"
    "v_add_f32 v27, v27, v49\n"
    "v_mfma_f32_16x16x32_bf16 v[36:39], v[72:75], a[8:11], v[36:39]\n"
    "v_exp_f32 v50, v50\n"
    "v_mfma_f32_16x16x32_bf16 v[36:39], v[76:79], a[12:15], v[36:39]\n"
    "v_add_f32 v27, v27, v50\n"
    "s_waitcnt lgkmcnt(0)\n"
    "v_mfma_f32_16x16x32_bf16 v[40:43], v[80:83], a[0:3], 0\n"
    "v_exp_f32 v51, v51\n"
    "v_mfma_f32_16x16x32_bf16 v[40:43], v[84:87], a[4:7], v[40:43]\n"
    "v_cvt_pk_bf16_f32 v59, v50, v51\n"
    "v_add_f32 v27, v27, v51\n"
    "v_mfma_f32_16x16x32_bf16 v[40:43], v[88:91], a[8:11], v[40:43]\n"
    "v_exp_f32 v36, v36\n"
    "v_mfma_f32_16x16x32_bf16 v[40:43], v[92:95], a[12:15], v[40:43]\n"
    "v_add_f32 v22, v22, v36\n"
    "s_mov_b32 m0, s76\n"
    "v_mfma_f32_16x16x32_bf16 v[168:171], v[96:99], v[56:59], v[168:171]\n"
    "s_waitcnt vmcnt(16)\n"
    "ds_read_b64_tr_b16 v[96:97], v8 offset:24576\n"
    "ds_read_b64_tr_b16 v[98:99], v8 offset:28672\n"
    "v_exp_f32 v37, v37\n"
    "v_mfma_f32_16x16x32_bf16 v[172:175], v[100:103], v[56:59], v[172:175]\n"
    "ds_read_b64_tr_b16 v[100:101], v9 offset:24576\n"
    "ds_read_b64_tr_b16 v[102:103], v9 offset:28672\n"
    "v_cvt_pk_bf16_f32 v52, v36, v37\n"
    "v_add_f32 v22, v22, v37\n"
    "v_mfma_f32_16x16x32_bf16 a[216:219], v[104:107], v[56:59], a[216:219]\n"
    "ds_read_b64_tr_b16 v[104:105], v10 offset:24576\n"
    "ds_read_b64_tr_b16 v[106:107], v10 offset:28672\n"
    "v_exp_f32 v38, v38\n"
    "v_mfma_f32_16x16x32_bf16 a[220:223], v[108:111], v[56:59], a[220:223]\n"
    "ds_read_b64_tr_b16 v[108:109], v11 offset:24576\n"
    "ds_read_b64_tr_b16 v[110:111], v11 offset:28672\n"
    "buffer_load_dwordx4 v16, s[40:43], s70 offen offset:0 lds\n"
    "v_add_f32 v22, v22, v38\n"
    "v_mfma_f32_16x16x32_bf16 a[224:227], v[112:115], v[56:59], a[224:227]\n"
    "ds_read_b64_tr_b16 v[112:113], v12 offset:24576\n"
    "ds_read_b64_tr_b16 v[114:115], v12 offset:28672\n"
    "v_exp_f32 v39, v39\n"
    "v_mfma_f32_16x16x32_bf16 a[228:231], v[116:119], v[56:59], a[228:231]\n"
    "ds_read_b64_tr_b16 v[116:117], v13 offset:24576\n"
    "ds_read_b64_tr_b16 v[118:119], v13 offset:28672\n"
    "v_cvt_pk_bf16_f32 v53, v38, v39\n"
    "v_add_f32 v22, v22, v39\n"
    "v_mfma_f32_16x16x32_bf16 a[232:235], v[120:123], v[56:59], a[232:235]\n"
    "ds_read_b64_tr_b16 v[120:121], v14 offset:24576\n"
    "ds_read_b64_tr_b16 v[122:123], v14 offset:28672\n"
    "v_exp_f32 v40, v40\n"
    "v_mfma_f32_16x16x32_bf16 a[236:239], v[124:127], v[56:59], a[236:239]\n"
    "ds_read_b64_tr_b16 v[124:125], v15 offset:24576\n"
    "ds_read_b64_tr_b16 v[126:127], v15 offset:28672\n"
    "buffer_load_dwordx4 v17, s[40:43], s70 offen offset:1024 lds\n"
    "v_add_f32 v22, v22, v40\n"
    "v_mfma_f32_16x16x32_bf16 v[44:47], v[64:67], a[16:19], 0\n"
    "v_exp_f32 v41, v41\n"
    "v_mfma_f32_16x16x32_bf16 v[44:47], v[68:71], a[20:23], v[44:47]\n"
    "v_cvt_pk_bf16_f32 v54, v40, v41\n"
    "v_add_f32 v22, v22, v41\n"
    "v_mfma_f32_16x16x32_bf16 v[44:47], v[72:75], a[24:27], v[44:47]\n"
    "v_exp_f32 v42, v42\n"
    "v_mfma_f32_16x16x32_bf16 v[44:47], v[76:79], a[28:31], v[44:47]\n"
    "buffer_load_dwordx4 v18, s[40:43], s70 offen offset:2048 lds\n"
    "v_add_f32 v22, v22, v42\n"
    "v_mfma_f32_16x16x32_bf16 v[48:51], v[80:83], a[16:19], 0\n"
    "buffer_load_dwordx4 v19, s[40:43], s70 offen offset:3072 lds\n"
    "v_exp_f32 v43, v43\n"
    "v_mfma_f32_16x16x32_bf16 v[48:51], v[84:87], a[20:23], v[48:51]\n"
    "v_cvt_pk_bf16_f32 v55, v42, v43\n"
    "v_add_f32 v22, v22, v43\n"
    "s_mov_b32 m0, s77\n"
    "v_mfma_f32_16x16x32_bf16 v[48:51], v[88:91], a[24:27], v[48:51]\n"
    "buffer_load_dwordx4 v16, s[40:43], s71 offen offset:0 lds\n"
    "v_exp_f32 v44, v44\n"
    "v_mfma_f32_16x16x32_bf16 v[48:51], v[92:95], a[28:31], v[48:51]\n"
    "v_add_f32 v23, v23, v44\n"
    "s_waitcnt lgkmcnt(8)\n"
    "v_mfma_f32_16x16x32_bf16 v[128:131], v[96:99], v[52:55], v[128:131]\n"
    "v_exp_f32 v45, v45\n"
    "v_mfma_f32_16x16x32_bf16 v[132:135], v[100:103], v[52:55], v[132:135]\n"
    "v_cvt_pk_bf16_f32 v56, v44, v45\n"
    "v_add_f32 v23, v23, v45\n"
    "v_mfma_f32_16x16x32_bf16 a[96:99], v[104:107], v[52:55], a[96:99]\n"
    "v_exp_f32 v46, v46\n"
    "v_mfma_f32_16x16x32_bf16 a[100:103], v[108:111], v[52:55], a[100:103]\n"
    "v_add_f32 v23, v23, v46\n"
    "s_waitcnt lgkmcnt(0)\n"
    "v_mfma_f32_16x16x32_bf16 a[104:107], v[112:115], v[52:55], a[104:107]\n"
    "v_exp_f32 v47, v47\n"
    "v_mfma_f32_16x16x32_bf16 a[108:111], v[116:119], v[52:55], a[108:111]\n"
    "v_cvt_pk_bf16_f32 v57, v46, v47\n"
    "v_add_f32 v23, v23, v47\n"
    "v_mfma_f32_16x16x32_bf16 a[112:115], v[120:123], v[52:55], a[112:115]\n"
    "v_exp_f32 v48, v48\n"
    "v_mfma_f32_16x16x32_bf16 a[116:119], v[124:127], v[52:55], a[116:119]\n"
    "buffer_load_dwordx4 v17, s[40:43], s71 offen offset:1024 lds\n"
    "v_add_f32 v23, v23, v48\n"
    "v_mfma_f32_16x16x32_bf16 v[36:39], v[64:67], a[32:35], 0\n"
    "v_exp_f32 v49, v49\n"
    "v_mfma_f32_16x16x32_bf16 v[36:39], v[68:71], a[36:39], v[36:39]\n"
    "v_cvt_pk_bf16_f32 v58, v48, v49\n"
    "v_add_f32 v23, v23, v49\n"
    "v_mfma_f32_16x16x32_bf16 v[36:39], v[72:75], a[40:43], v[36:39]\n"
    "v_exp_f32 v50, v50\n"
    "v_mfma_f32_16x16x32_bf16 v[36:39], v[76:79], a[44:47], v[36:39]\n"
    "buffer_load_dwordx4 v18, s[40:43], s71 offen offset:2048 lds\n"
    "v_add_f32 v23, v23, v50\n"
    "v_mfma_f32_16x16x32_bf16 v[40:43], v[80:83], a[32:35], 0\n"
    "v_exp_f32 v51, v51\n"
    "v_mfma_f32_16x16x32_bf16 v[40:43], v[84:87], a[36:39], v[40:43]\n"
    "v_cvt_pk_bf16_f32 v59, v50, v51\n"
    "v_add_f32 v23, v23, v51\n"
    "v_mfma_f32_16x16x32_bf16 v[40:43], v[88:91], a[40:43], v[40:43]\n"
    "v_exp_f32 v36, v36\n"
    "v_mfma_f32_16x16x32_bf16 v[40:43], v[92:95], a[44:47], v[40:43]\n"
    "buffer_load_dwordx4 v19, s[40:43], s71 offen offset:3072 lds\n"
    "v_add_f32 v24, v24, v36\n"
    "v_mfma_f32_16x16x32_bf16 v[136:139], v[96:99], v[56:59], v[136:139]\n"
    "v_exp_f32 v37, v37\n"
    "s_mov_b32 m0, s78\n"
    "v_mfma_f32_16x16x32_bf16 v[140:143], v[100:103], v[56:59], v[140:143]\n"
    "v_cvt_pk_bf16_f32 v52, v36, v37\n"
    "v_add_f32 v24, v24, v37\n"
    "v_mfma_f32_16x16x32_bf16 a[120:123], v[104:107], v[56:59], a[120:123]\n"
    "v_exp_f32 v38, v38\n"
    "v_mfma_f32_16x16x32_bf16 a[124:127], v[108:111], v[56:59], a[124:127]\n"
    "buffer_load_dwordx4 v20, s[44:47], s70 offen offset:0 lds\n"
    "v_add_f32 v24, v24, v38\n"
    "v_mfma_f32_16x16x32_bf16 a[128:131], v[112:115], v[56:59], a[128:131]\n"
    "v_exp_f32 v39, v39\n"
    "v_mfma_f32_16x16x32_bf16 a[132:135], v[116:119], v[56:59], a[132:135]\n"
    "v_cvt_pk_bf16_f32 v53, v38, v39\n"
    "v_add_f32 v24, v24, v39\n"
    "v_mfma_f32_16x16x32_bf16 a[136:139], v[120:123], v[56:59], a[136:139]\n"
    "v_exp_f32 v40, v40\n"
    "v_mfma_f32_16x16x32_bf16 a[140:143], v[124:127], v[56:59], a[140:143]\n"
    "buffer_load_dwordx4 v21, s[44:47], s70 offen offset:1024 lds\n"
    "v_add_f32 v24, v24, v40\n"
    "v_mfma_f32_16x16x32_bf16 v[44:47], v[64:67], a[48:51], 0\n"
    "v_exp_f32 v41, v41\n"
    "v_mfma_f32_16x16x32_bf16 v[44:47], v[68:71], a[52:55], v[44:47]\n"
    "v_cvt_pk_bf16_f32 v54, v40, v41\n"
    "v_add_f32 v24, v24, v41\n"
    "v_mfma_f32_16x16x32_bf16 v[44:47], v[72:75], a[56:59], v[44:47]\n"
    "v_exp_f32 v42, v42\n"
    "v_mfma_f32_16x16x32_bf16 v[44:47], v[76:79], a[60:63], v[44:47]\n"
    "buffer_load_dwordx4 v20, s[44:47], s70 offen offset:2048 lds\n"
    "v_add_f32 v24, v24, v42\n"
    "v_mfma_f32_16x16x32_bf16 v[48:51], v[80:83], a[48:51], 0\n"
    "v_exp_f32 v43, v43\n"
    "v_mfma_f32_16x16x32_bf16 v[48:51], v[84:87], a[52:55], v[48:51]\n"
    "v_cvt_pk_bf16_f32 v55, v42, v43\n"
    "v_add_f32 v24, v24, v43\n"
    "v_mfma_f32_16x16x32_bf16 v[48:51], v[88:91], a[56:59], v[48:51]\n"
    "v_exp_f32 v44, v44\n"
    "v_mfma_f32_16x16x32_bf16 v[48:51], v[92:95], a[60:63], v[48:51]\n"
    "buffer_load_dwordx4 v21, s[44:47], s70 offen offset:3072 lds\n"
    "v_add_f32 v25, v25, v44\n"
    "v_mfma_f32_16x16x32_bf16 v[144:147], v[96:99], v[52:55], v[144:147]\n"
    "v_exp_f32 v45, v45\n"
    "s_mov_b32 m0, s79\n"
    "v_mfma_f32_16x16x32_bf16 v[148:151], v[100:103], v[52:55], v[148:151]\n"
    "v_cvt_pk_bf16_f32 v56, v44, v45\n"
    "v_add_f32 v25, v25, v45\n"
    "v_mfma_f32_16x16x32_bf16 a[144:147], v[104:107], v[52:55], a[144:147]\n"
    "v_exp_f32 v46, v46\n"
    "v_mfma_f32_16x16x32_bf16 a[148:151], v[108:111], v[52:55], a[148:151]\n"
    "buffer_load_dwordx4 v20, s[44:47], s71 offen offset:0 lds\n"
    "v_add_f32 v25, v25, v46\n"
    "v_mfma_f32_16x16x32_bf16 a[152:155], v[112:115], v[52:55], a[152:155]\n"
    "v_exp_f32 v47, v47\n"
    "v_mfma_f32_16x16x32_bf16 a[156:159], v[116:119], v[52:55], a[156:159]\n"
    "v_cvt_pk_bf16_f32 v57, v46, v47\n"
    "v_add_f32 v25, v25, v47\n"
    "v_mfma_f32_16x16x32_bf16 a[160:163], v[120:123], v[52:55], a[160:163]\n"
    "v_exp_f32 v48, v48\n"
    "v_mfma_f32_16x16x32_bf16 a[164:167], v[124:127], v[52:55], a[164:167]\n"
    "buffer_load_dwordx4 v21, s[44:47], s71 offen offset:1024 lds\n"
    "v_add_f32 v25, v25, v48\n"
    "v_mfma_f32_16x16x32_bf16 v[36:39], v[64:67], a[64:67], 0\n"
    "v_exp_f32 v49, v49\n"
    "v_mfma_f32_16x16x32_bf16 v[36:39], v[68:71], a[68:71], v[36:39]\n"
    "v_cvt_pk_bf16_f32 v58, v48, v49\n"
    "v_add_f32 v25, v25, v49\n"
    "v_mfma_f32_16x16x32_bf16 v[36:39], v[72:75], a[72:75], v[36:39]\n"
    "v_exp_f32 v50, v50\n"
    "v_mfma_f32_16x16x32_bf16 v[36:39], v[76:79], a[76:79], v[36:39]\n"
    "buffer_load_dwordx4 v20, s[44:47], s71 offen offset:2048 lds\n"
    "v_add_f32 v25, v25, v50\n"
    "v_mfma_f32_16x16x32_bf16 v[40:43], v[80:83], a[64:67], 0\n"
    "v_exp_f32 v51, v51\n"
    "v_mfma_f32_16x16x32_bf16 v[40:43], v[84:87], a[68:71], v[40:43]\n"
    "v_cvt_pk_bf16_f32 v59, v50, v51\n"
    "v_add_f32 v25, v25, v51\n"
    "v_mfma_f32_16x16x32_bf16 v[40:43], v[88:91], a[72:75], v[40:43]\n"
    "v_exp_f32 v36, v36\n"
    "v_mfma_f32_16x16x32_bf16 v[40:43], v[92:95], a[76:79], v[40:43]\n"
    "buffer_load_dwordx4 v21, s[44:47], s71 offen offset:3072 lds\n"
    "v_add_f32 v26, v26, v36\n"
    "v_mfma_f32_16x16x32_bf16 v[152:155], v[96:99], v[56:59], v[152:155]\n"
    "v_exp_f32 v37, v37\n"
    "v_mfma_f32_16x16x32_bf16 v[156:159], v[100:103], v[56:59], v[156:159]\n"
    "v_cvt_pk_bf16_f32 v52, v36, v37\n"
    "v_add_f32 v26, v26, v37\n"
    "v_mfma_f32_16x16x32_bf16 a[168:171], v[104:107], v[56:59], a[168:171]\n"
    "v_exp_f32 v38, v38\n"
    "v_add_u32 v16, s94, v30\n"
    "v_mfma_f32_16x16x32_bf16 a[172:175], v[108:111], v[56:59], a[172:175]\n"
    "v_add_f32 v26, v26, v38\n"
    "s_lshl_b32 s80, s36, 12\n"
    "v_mfma_f32_16x16x32_bf16 a[176:179], v[112:115], v[56:59], a[176:179]\n"
    "v_exp_f32 v39, v39\n"
    "s_add_u32 s80, s80, 131072\n"
    "v_mfma_f32_16x16x32_bf16 a[180:183], v[116:119], v[56:59], a[180:183]\n"
    "v_cvt_pk_bf16_f32 v53, v38, v39\n"
    "v_add_f32 v26, v26, v39\n"
    "s_add_u32 s80, s80, %[lds]\n"
    "v_mfma_f32_16x16x32_bf16 a[184:187], v[120:123], v[56:59], a[184:187]\n"
    "v_exp_f32 v40, v40\n"
    "v_add_u32 v17, s80, v30\n"
    "v_mfma_f32_16x16x32_bf16 a[188:191], v[124:127], v[56:59], a[188:191]\n"
    "v_add_f32 v26, v26, v40\n"
    "v_and_b32 v28, 15, v29\n"
    "v_mfma_f32_16x16x32_bf16 v[44:47], v[64:67], a[80:83], 0\n"
    "s_waitcnt vmcnt(24)\n"
    "ds_read_b128 v[64:67], v4 offset:0\n"
    "v_exp_f32 v41, v41\n"
    "s_add_u32 s80, %[lds], 155648\n"
    "v_mfma_f32_16x16x32_bf16 v[44:47], v[68:71], a[84:87], v[44:47]\n"
    "ds_read_b128 v[68:71], v5 offset:0\n"
    "v_cvt_pk_bf16_f32 v54, v40, v41\n"
    "v_add_f32 v26, v26, v41\n"
    "v_lshl_add_u32 v19, v28, 4, s80\n"
    "v_mfma_f32_16x16x32_bf16 v[44:47], v[72:75], a[88:91], v[44:47]\n"
    "ds_read_b128 v[72:75], v6 offset:0\n"
    "v_exp_f32 v42, v42\n"
    "s_lshl_b32 s81, s36, 2\n"
    "v_mfma_f32_16x16x32_bf16 v[44:47], v[76:79], a[92:95], v[44:47]\n"
    "ds_read_b128 v[76:79], v7 offset:0\n"
    "v_add_f32 v26, v26, v42\n"
    "v_add_u32 v18, s81, v19\n"
    "v_mfma_f32_16x16x32_bf16 v[48:51], v[80:83], a[80:83], 0\n"
    "ds_read_b128 v[80:83], v4 offset:4096\n"
    "v_exp_f32 v43, v43\n"
    "v_bfe_u32 v31, v29, 4, 1\n"
    "v_mfma_f32_16x16x32_bf16 v[48:51], v[84:87], a[84:87], v[48:51]\n"
    "ds_read_b128 v[84:87], v5 offset:4096\n"
    "v_cvt_pk_bf16_f32 v55, v42, v43\n"
    "v_add_f32 v26, v26, v43\n"
    "v_lshlrev_b32 v31, 5, v31\n"
    "v_mfma_f32_16x16x32_bf16 v[48:51], v[88:91], a[88:91], v[48:51]\n"
    "ds_read_b128 v[88:91], v6 offset:4096\n"
    "v_exp_f32 v44, v44\n"
    "v_lshl_or_b32 v20, v28, 8, v31\n"
    "v_mfma_f32_16x16x32_bf16 v[48:51], v[92:95], a[92:95], v[48:51]\n"
    "ds_read_b128 v[92:95], v7 offset:4096\n"
    "v_add_f32 v27, v27, v44\n"
    "v_lshrrev_b32 v31, 5, v29\n"
    "v_mfma_f32_16x16x32_bf16 v[160:163], v[96:99], v[52:55], v[160:163]\n"
    "v_exp_f32 v45, v45\n"
    "v_lshl_or_b32 v20, v31, 4, v20\n"
    "v_mfma_f32_16x16x32_bf16 v[164:167], v[100:103], v[52:55], v[164:167]\n"
    "v_cvt_pk_bf16_f32 v56, v44, v45\n"
    "v_add_f32 v27, v27, v45\n"
    "s_lshl_b32 s81, s36, 6\n"
    "v_mfma_f32_16x16x32_bf16 a[192:195], v[104:107], v[52:55], a[192:195]\n"
    "v_exp_f32 v46, v46\n"
    "v_or_b32 v20, s81, v20\n"
    "v_mfma_f32_16x16x32_bf16 a[196:199], v[108:111], v[52:55], a[196:199]\n"
    "v_add_f32 v27, v27, v46\n"
    "v_accvgpr_write_b32 a240, 0\n"
    "v_mfma_f32_16x16x32_bf16 a[200:203], v[112:115], v[52:55], a[200:203]\n"
    "s_waitcnt vmcnt(8)\n"
    "ds_read_b128 v[176:179], v4 offset:16384\n"
    "v_exp_f32 v47, v47\n"
    "v_accvgpr_write_b32 a241, 0\n"
    "v_mfma_f32_16x16x32_bf16 a[204:207], v[116:119], v[52:55], a[204:207]\n"
    "ds_read_b128 v[180:183], v5 offset:16384\n"
    "v_cvt_pk_bf16_f32 v57, v46, v47\n"
    "v_add_f32 v27, v27, v47\n"
    "v_accvgpr_write_b32 a242, 0\n"
    "v_mfma_f32_16x16x32_bf16 a[208:211], v[120:123], v[52:55], a[208:211]\n"
    "ds_read_b128 v[184:187], v6 offset:16384\n"
    "v_exp_f32 v48, v48\n"
    "v_accvgpr_write_b32 a243, 0\n"
    "v_mfma_f32_16x16x32_bf16 a[212:215], v[124:127], v[52:55], a[212:215]\n"
    "ds_read_b128 v[188:191], v7 offset:16384\n"
    "v_add_f32 v27, v27, v48\n"
    "s_waitcnt lgkmcnt(8)\n"
    "v_mfma_f32_16x16x32_bf16 v[36:39], v[64:67], a[0:3], 0\n"
    "ds_read_b128 v[192:195], v4 offset:20480\n"
    "v_exp_f32 v49, v49\n"
    "v_mfma_f32_16x16x32_bf16 v[36:39], v[68:71], a[4:7], v[36:39]\n"
    "ds_read_b128 v[196:199], v5 offset:20480\n"
    "v_cvt_pk_bf16_f32 v58, v48, v49\n"
    "v_add_f32 v27, v27, v49\n"
    "v_mfma_f32_16x16x32_bf16 v[36:39], v[72:75], a[8:11], v[36:39]\n"
    "ds_read_b128 v[200:203], v6 offset:20480\n"
    "v_exp_f32 v50, v50\n"
    "v_mfma_f32_16x16x32_bf16 v[36:39], v[76:79], a[12:15], v[36:39]\n"
    "ds_read_b128 v[204:207], v7 offset:20480\n"
    "v_add_f32 v27, v27, v50\n"
    "s_waitcnt lgkmcnt(8)\n"
    "v_mfma_f32_16x16x32_bf16 v[40:43], v[80:83], a[0:3], 0\n"
    "v_exp_f32 v51, v51\n"
    "v_mfma_f32_16x16x32_bf16 v[40:43], v[84:87], a[4:7], v[40:43]\n"
    "v_cvt_pk_bf16_f32 v59, v50, v51\n"
    "v_add_f32 v27, v27, v51\n"
    "s_xor_b32 s81, s36, 1\n"
    "v_mfma_f32_16x16x32_bf16 v[40:43], v[88:91], a[8:11], v[40:43]\n"
    "v_exp_f32 v36, v36\n"
    "s_lshl_b32 s82, s81, 15\n"
    "v_mfma_f32_16x16x32_bf16 v[40:43], v[92:95], a[12:15], v[40:43]\n"
    "v_add_f32 v22, v22, v36\n"
    "s_add_u32 s82, s82, %[lds]\n"
    "v_mfma_f32_16x16x32_bf16 v[168:171], v[96:99], v[56:59], v[168:171]\n"
    "ds_read_b64_tr_b16 v[96:97], v8 offset:8192\n"
    "ds_read_b64_tr_b16 v[98:99], v8 offset:12288\n"
    "v_exp_f32 v37, v37\n"
    "v_add_u32 v4, s82, v30\n"
    "v_mfma_f32_16x16x32_bf16 v[172:175], v[100:103], v[56:59], v[172:175]\n"
    "ds_read_b64_tr_b16 v[100:101], v9 offset:8192\n"
    "ds_read_b64_tr_b16 v[102:103], v9 offset:12288\n"
    "v_cvt_pk_bf16_f32 v52, v36, v37\n"
    "v_add_f32 v22, v22, v37\n"
    "s_xor_b32 s81, s36, 2\n"
    "v_mfma_f32_16x16x32_bf16 a[216:219], v[104:107], v[56:59], a[216:219]\n"
    "ds_read_b64_tr_b16 v[104:105], v10 offset:8192\n"
    "ds_read_b64_tr_b16 v[106:107], v10 offset:12288\n"
    "v_exp_f32 v38, v38\n"
    "s_lshl_b32 s82, s81, 15\n"
    "v_mfma_f32_16x16x32_bf16 a[220:223], v[108:111], v[56:59], a[220:223]\n"
    "ds_read_b64_tr_b16 v[108:109], v11 offset:8192\n"
    "ds_read_b64_tr_b16 v[110:111], v11 offset:12288\n"
    "v_add_f32 v22, v22, v38\n"
    "s_add_u32 s82, s82, %[lds]\n"
    "v_mfma_f32_16x16x32_bf16 a[224:227], v[112:115], v[56:59], a[224:227]\n"
    "ds_read_b64_tr_b16 v[112:113], v12 offset:8192\n"
    "ds_read_b64_tr_b16 v[114:115], v12 offset:12288\n"
    "v_exp_f32 v39, v39\n"
    "v_add_u32 v5, s82, v30\n"
    "v_mfma_f32_16x16x32_bf16 a[228:231], v[116:119], v[56:59], a[228:231]\n"
    "ds_read_b64_tr_b16 v[116:117], v13 offset:8192\n"
    "ds_read_b64_tr_b16 v[118:119], v13 offset:12288\n"
    "v_cvt_pk_bf16_f32 v53, v38, v39\n"
    "v_add_f32 v22, v22, v39\n"
    "s_xor_b32 s81, s36, 3\n"
    "v_mfma_f32_16x16x32_bf16 a[232:235], v[120:123], v[56:59], a[232:235]\n"
    "ds_read_b64_tr_b16 v[120:121], v14 offset:8192\n"
    "ds_read_b64_tr_b16 v[122:123], v14 offset:12288\n"
    "v_exp_f32 v40, v40\n"
    "s_lshl_b32 s82, s81, 15\n"
    "v_mfma_f32_16x16x32_bf16 a[236:239], v[124:127], v[56:59], a[236:239]\n"
    "ds_read_b64_tr_b16 v[124:125], v15 offset:8192\n"
    "ds_read_b64_tr_b16 v[126:127], v15 offset:12288\n"
    "v_add_f32 v22, v22, v40\n"
    "s_add_u32 s82, s82, %[lds]\n"
    "s_waitcnt lgkmcnt(14)\n"
    "v_mfma_f32_16x16x32_bf16 v[44:47], v[176:179], a[0:3], 0\n"
    "s_waitcnt vmcnt(0)\n"
    "ds_read_b64_tr_b16 v[216:217], v10 offset:24576\n"
    "ds_read_b64_tr_b16 v[218:219], v10 offset:28672\n"
    "v_exp_f32 v41, v41\n"
    "v_add_u32 v6, s82, v30\n"
    "v_mfma_f32_16x16x32_bf16 v[44:47], v[180:183], a[4:7], v[44:47]\n"
    "ds_read_b64_tr_b16 v[220:221], v11 offset:24576\n"
    "ds_read_b64_tr_b16 v[222:223], v11 offset:28672\n"
    "v_cvt_pk_bf16_f32 v54, v40, v41\n"
    "v_add_f32 v22, v22, v41\n"
    "s_lshl_b32 s82, s81, 12\n"
    "v_mfma_f32_16x16x32_bf16 v[44:47], v[184:187], a[8:11], v[44:47]\n"
    "ds_read_b64_tr_b16 v[224:225], v12 offset:24576\n"
    "ds_read_b64_tr_b16 v[226:227], v12 offset:28672\n"
    "v_exp_f32 v42, v42\n"
    "s_add_u32 s82, s82, 131072\n"
    "v_mfma_f32_16x16x32_bf16 v[44:47], v[188:191], a[12:15], v[44:47]\n"
    "ds_read_b64_tr_b16 v[228:229], v13 offset:24576\n"
    "ds_read_b64_tr_b16 v[230:231], v13 offset:28672\n"
    "v_add_f32 v22, v22, v42\n"
    "s_add_u32 s82, s82, %[lds]\n"
    "v_mfma_f32_16x16x32_bf16 v[48:51], v[192:195], a[0:3], 0\n"
    "ds_read_b64_tr_b16 v[232:233], v14 offset:24576\n"
    "ds_read_b64_tr_b16 v[234:235], v14 offset:28672\n"
    "v_exp_f32 v43, v43\n"
    "v_add_u32 v7, s82, v30\n"
    "v_mfma_f32_16x16x32_bf16 v[48:51], v[196:199], a[4:7], v[48:51]\n"
    "ds_read_b64_tr_b16 v[236:237], v15 offset:24576\n"
    "ds_read_b64_tr_b16 v[238:239], v15 offset:28672\n"
    "v_cvt_pk_bf16_f32 v55, v42, v43\n"
    "v_add_f32 v22, v22, v43\n"
    "s_xor_b32 s81, s36, 2\n"
    "v_mfma_f32_16x16x32_bf16 v[48:51], v[200:203], a[8:11], v[48:51]\n"
    "ds_read_b64_tr_b16 v[208:209], v8 offset:24576\n"
    "ds_read_b64_tr_b16 v[210:211], v8 offset:28672\n"
    "v_exp_f32 v44, v44\n"
    "s_lshl_b32 s82, s81, 12\n"
    "v_mfma_f32_16x16x32_bf16 v[48:51], v[204:207], a[12:15], v[48:51]\n"
    "ds_read_b64_tr_b16 v[212:213], v9 offset:24576\n"
    "ds_read_b64_tr_b16 v[214:215], v9 offset:28672\n"
    "v_add_f32 v22, v22, v44\n"
    "s_add_u32 s82, s82, 131072\n"
    "s_waitcnt lgkmcnt(14)\n"
    "v_mfma_f32_16x16x32_bf16 v[128:131], v[96:99], v[52:55], v[128:131]\n"
    "v_exp_f32 v45, v45\n"
    "s_add_u32 s82, s82, %[lds]\n"
    "v_mfma_f32_16x16x32_bf16 v[132:135], v[100:103], v[52:55], v[132:135]\n"
    "v_cvt_pk_bf16_f32 v56, v44, v45\n"
    "v_add_f32 v22, v22, v45\n"
    "v_add_u32 v32, s82, v30\n"
    "v_mfma_f32_16x16x32_bf16 a[96:99], v[104:107], v[52:55], a[96:99]\n"
    "v_exp_f32 v46, v46\n"
    "v_mfma_f32_16x16x32_bf16 a[100:103], v[108:111], v[52:55], a[100:103]\n"
    "v_add_f32 v22, v22, v46\n"
    "v_mfma_f32_16x16x32_bf16 a[104:107], v[112:115], v[52:55], a[104:107]\n"
    "v_exp_f32 v47, v47\n"
    "v_mfma_f32_16x16x32_bf16 a[108:111], v[116:119], v[52:55], a[108:111]\n"
    "v_cvt_pk_bf16_f32 v57, v46, v47\n"
    "v_add_f32 v22, v22, v47\n"
    "v_mfma_f32_16x16x32_bf16 a[112:115], v[120:123], v[52:55], a[112:115]\n"
    "v_exp_f32 v48, v48\n"
    "v_mfma_f32_16x16x32_bf16 a[116:119], v[124:127], v[52:55], a[116:119]\n"
    "v_add_f32 v22, v22, v48\n"
    "v_mfma_f32_16x16x32_bf16 v[36:39], v[64:67], a[16:19], 0\n"
    "v_exp_f32 v49, v49\n"
    "v_mfma_f32_16x16x32_bf16 v[36:39], v[68:71], a[20:23], v[36:39]\n"
    "v_cvt_pk_bf16_f32 v58, v48, v49\n"
    "v_add_f32 v22, v22, v49\n"
    "v_mfma_f32_16x16x32_bf16 v[36:39], v[72:75], a[24:27], v[36:39]\n"
    "v_exp_f32 v50, v50\n"
    "v_mfma_f32_16x16x32_bf16 v[36:39], v[76:79], a[28:31], v[36:39]\n"
    "v_add_f32 v22, v22, v50\n"
    "v_mfma_f32_16x16x32_bf16 v[40:43], v[80:83], a[16:19], 0\n"
    "v_exp_f32 v51, v51\n"
    "v_mfma_f32_16x16x32_bf16 v[40:43], v[84:87], a[20:23], v[40:43]\n"
    "v_cvt_pk_bf16_f32 v59, v50, v51\n"
    "v_add_f32 v22, v22, v51\n"
    "v_mfma_f32_16x16x32_bf16 v[40:43], v[88:91], a[24:27], v[40:43]\n"
    "s_nop 0\n"
    "v_exp_f32 v36, v36\n"
    "v_mov_b32 v28, v22\n"
    "v_mfma_f32_16x16x32_bf16 v[40:43], v[92:95], a[28:31], v[40:43]\n"
    "v_add_f32 v23, v23, v36\n"
    "v_permlane32_swap_b32 v28, v22\n"
    "v_mfma_f32_16x16x32_bf16 a[96:99], v[216:219], v[56:59], a[96:99]\n"
    "v_exp_f32 v37, v37\n"
    "v_add_f32 v22, v22, v28\n"
    "s_waitcnt lgkmcnt(12)\n"
    "v_mfma_f32_16x16x32_bf16 a[100:103], v[220:223], v[56:59], a[100:103]\n"
    "v_cvt_pk_bf16_f32 v52, v36, v37\n"
    "v_add_f32 v23, v23, v37\n"
    "v_mov_b32 v28, v22\n"
    "s_waitcnt lgkmcnt(10)\n"
    "v_mfma_f32_16x16x32_bf16 a[104:107], v[224:227], v[56:59], a[104:107]\n"
    "v_exp_f32 v38, v38\n"
    "v_permlane16_swap_b32 v28, v22\n"
    "s_waitcnt lgkmcnt(8)\n"
    "v_mfma_f32_16x16x32_bf16 a[108:111], v[228:231], v[56:59], a[108:111]\n"
    "v_add_f32 v23, v23, v38\n"
    "v_add_f32 v22, v22, v28\n"
    "s_waitcnt lgkmcnt(6)\n"
    "v_mfma_f32_16x16x32_bf16 a[112:115], v[232:235], v[56:59], a[112:115]\n"
    "v_exp_f32 v39, v39\n"
    "ds_write_b32 v18, v22 offset:0\n"
    "s_waitcnt lgkmcnt(5)\n"
    "v_mfma_f32_16x16x32_bf16 a[116:119], v[236:239], v[56:59], a[116:119]\n"
    "v_cvt_pk_bf16_f32 v53, v38, v39\n"
    "v_add_f32 v23, v23, v39\n"
    "s_waitcnt lgkmcnt(3)\n"
    "v_mfma_f32_16x16x32_bf16 v[128:131], v[208:211], v[56:59], v[128:131]\n"
    "ds_write_b128 v16, a[96:99] offset:0\n"
    "v_exp_f32 v40, v40\n"
    "s_waitcnt lgkmcnt(2)\n"
    "v_mfma_f32_16x16x32_bf16 v[132:135], v[212:215], v[56:59], v[132:135]\n"
    "v_add_f32 v23, v23, v40\n"
    "v_mfma_f32_16x16x32_bf16 v[44:47], v[176:179], a[16:19], 0\n"
    "v_exp_f32 v41, v41\n"
    "v_mfma_f32_16x16x32_bf16 v[44:47], v[180:183], a[20:23], v[44:47]\n"
    "v_cvt_pk_bf16_f32 v54, v40, v41\n"
    "v_add_f32 v23, v23, v41\n"
    "v_mfma_f32_16x16x32_bf16 v[44:47], v[184:187], a[24:27], v[44:47]\n"
    "ds_write_b128 v16, a[100:103] offset:1024\n"
    "v_exp_f32 v42, v42\n"
    "v_mfma_f32_16x16x32_bf16 v[44:47], v[188:191], a[28:31], v[44:47]\n"
    "v_add_f32 v23, v23, v42\n"
    "v_mfma_f32_16x16x32_bf16 v[48:51], v[192:195], a[16:19], 0\n"
    "v_exp_f32 v43, v43\n"
    "v_mfma_f32_16x16x32_bf16 v[48:51], v[196:199], a[20:23], v[48:51]\n"
    "v_cvt_pk_bf16_f32 v55, v42, v43\n"
    "v_add_f32 v23, v23, v43\n"
    "v_mfma_f32_16x16x32_bf16 v[48:51], v[200:203], a[24:27], v[48:51]\n"
    "ds_write_b128 v16, a[104:107] offset:2048\n"
    "v_exp_f32 v44, v44\n"
    "v_mfma_f32_16x16x32_bf16 v[48:51], v[204:207], a[28:31], v[48:51]\n"
    "v_add_f32 v23, v23, v44\n"
    "v_mfma_f32_16x16x32_bf16 v[136:139], v[96:99], v[52:55], v[136:139]\n"
    "v_exp_f32 v45, v45\n"
    "v_mfma_f32_16x16x32_bf16 v[140:143], v[100:103], v[52:55], v[140:143]\n"
    "v_cvt_pk_bf16_f32 v56, v44, v45\n"
    "v_add_f32 v23, v23, v45\n"
    "v_mfma_f32_16x16x32_bf16 a[120:123], v[104:107], v[52:55], a[120:123]\n"
    "ds_write_b128 v16, a[108:111] offset:3072\n"
    "v_exp_f32 v46, v46\n"
    "v_mfma_f32_16x16x32_bf16 a[124:127], v[108:111], v[52:55], a[124:127]\n"
    "v_add_f32 v23, v23, v46\n"
    "v_mfma_f32_16x16x32_bf16 a[128:131], v[112:115], v[52:55], a[128:131]\n"
    "v_exp_f32 v47, v47\n"
    "v_mfma_f32_16x16x32_bf16 a[132:135], v[116:119], v[52:55], a[132:135]\n"
    "v_cvt_pk_bf16_f32 v57, v46, v47\n"
    "v_add_f32 v23, v23, v47\n"
    "v_mfma_f32_16x16x32_bf16 a[136:139], v[120:123], v[52:55], a[136:139]\n"
    "ds_write_b128 v16, a[112:115] offset:4096\n"
    "v_exp_f32 v48, v48\n"
    "v_mfma_f32_16x16x32_bf16 a[140:143], v[124:127], v[52:55], a[140:143]\n"
    "v_add_f32 v23, v23, v48\n"
    "v_mfma_f32_16x16x32_bf16 v[36:39], v[64:67], a[32:35], 0\n"
    "v_exp_f32 v49, v49\n"
    "v_mfma_f32_16x16x32_bf16 v[36:39], v[68:71], a[36:39], v[36:39]\n"
    "v_cvt_pk_bf16_f32 v58, v48, v49\n"
    "v_add_f32 v23, v23, v49\n"
    "v_mfma_f32_16x16x32_bf16 v[36:39], v[72:75], a[40:43], v[36:39]\n"
    "ds_write_b128 v16, a[116:119] offset:5120\n"
    "v_exp_f32 v50, v50\n"
    "v_mfma_f32_16x16x32_bf16 v[36:39], v[76:79], a[44:47], v[36:39]\n"
    "v_add_f32 v23, v23, v50\n"
    "v_mfma_f32_16x16x32_bf16 v[40:43], v[80:83], a[32:35], 0\n"
    "v_exp_f32 v51, v51\n"
    "v_mfma_f32_16x16x32_bf16 v[40:43], v[84:87], a[36:39], v[40:43]\n"
    "v_cvt_pk_bf16_f32 v59, v50, v51\n"
    "v_add_f32 v23, v23, v51\n"
    "s_waitcnt lgkmcnt(0)\n"
    "s_barrier\n"
    "v_mfma_f32_16x16x32_bf16 v[40:43], v[88:91], a[40:43], v[40:43]\n"
    "ds_read_b128 v[60:63], v19 offset:0\n"
    "v_exp_f32 v36, v36\n"
    "v_mov_b32 v28, v23\n"
    "v_mfma_f32_16x16x32_bf16 v[40:43], v[92:95], a[44:47], v[40:43]\n"
    "ds_read_b128 v[240:243], v4 offset:0\n"
    "v_add_f32 v24, v24, v36\n"
    "v_permlane32_swap_b32 v28, v23\n"
    "v_mfma_f32_16x16x32_bf16 a[120:123], v[216:219], v[56:59], a[120:123]\n"
    "ds_read_b128 v[244:247], v5 offset:2048\n"
    "v_exp_f32 v37, v37\n"
    "v_add_f32 v23, v23, v28\n"
    "v_mfma_f32_16x16x32_bf16 a[124:127], v[220:223], v[56:59], a[124:127]\n"
    "ds_read_b128 v[248:251], v6 offset:4096\n"
    "v_cvt_pk_bf16_f32 v52, v36, v37\n"
    "v_add_f32 v24, v24, v37\n"
    "v_mov_b32 v28, v23\n"
    "v_mfma_f32_16x16x32_bf16 a[128:131], v[224:227], v[56:59], a[128:131]\n"
    "ds_read_b128 v[252:255], v4 offset:1024\n"
    "v_exp_f32 v38, v38\n"
    "v_permlane16_swap_b32 v28, v23\n"
    "v_mfma_f32_16x16x32_bf16 a[132:135], v[228:231], v[56:59], a[132:135]\n"
    "ds_read_b128 v[8:11], v5 offset:3072\n"
    "v_add_f32 v24, v24, v38\n"
    "v_add_f32 v23, v23, v28\n"
    "v_mfma_f32_16x16x32_bf16 a[136:139], v[232:235], v[56:59], a[136:139]\n"
    "ds_read_b128 v[12:15], v6 offset:5120\n"
    "v_exp_f32 v39, v39\n"
    "ds_write_b32 v18, v23 offset:256\n"
    "v_mfma_f32_16x16x32_bf16 a[140:143], v[236:239], v[56:59], a[140:143]\n"
    "v_cvt_pk_bf16_f32 v53, v38, v39\n"
    "v_add_f32 v24, v24, v39\n"
    "s_waitcnt lgkmcnt(1)\n"
    "v_mfma_f32_16x16x32_bf16 v[136:139], v[208:211], v[56:59], v[136:139]\n"
    "ds_write_b128 v16, a[120:123] offset:6144\n"
    "v_exp_f32 v40, v40\n"
    "v_add_f32 v60, v60, v61\n"
    "v_mfma_f32_16x16x32_bf16 v[140:143], v[212:215], v[56:59], v[140:143]\n"
    "v_add_f32 v24, v24, v40\n"
    "v_add_f32 v62, v62, v63\n"
    "v_mfma_f32_16x16x32_bf16 v[44:47], v[176:179], a[32:35], 0\n"
    "v_exp_f32 v41, v41\n"
    "v_add_f32 v60, v60, v62\n"
    "v_mfma_f32_16x16x32_bf16 v[44:47], v[180:183], a[36:39], v[44:47]\n"
    "v_cvt_pk_bf16_f32 v54, v40, v41\n"
    "v_add_f32 v24, v24, v41\n"
    "v_rcp_f32 v21, v60\n"
    "v_mfma_f32_16x16x32_bf16 v[44:47], v[184:187], a[40:43], v[44:47]\n"
    "ds_write_b128 v16, a[124:127] offset:7168\n"
    "v_exp_f32 v42, v42\n"
    "v_mfma_f32_16x16x32_bf16 v[44:47], v[188:191], a[44:47], v[44:47]\n"
    "v_add_f32 v24, v24, v42\n"
    "v_mfma_f32_16x16x32_bf16 v[48:51], v[192:195], a[32:35], 0\n"
    "v_exp_f32 v43, v43\n"
    "v_pk_add_f32 v[128:129], v[128:129], v[240:241]\n"
    "v_pk_add_f32 v[244:245], v[244:245], v[248:249]\n"
    "v_cmp_nle_f32 vcc, s95, v60\n"
    "v_pk_add_f32 v[130:131], v[130:131], v[242:243]\n"
    "v_pk_add_f32 v[246:247], v[246:247], v[250:251]\n"
    "v_pk_add_f32 v[128:129], v[128:129], v[244:245]\n"
    "v_pk_add_f32 v[130:131], v[130:131], v[246:247]\n"
    "v_pk_mul_f32 v[128:129], v[128:129], v[20:21] op_sel:[0,1] op_sel_hi:[1,1]\n"
    "s_or_b64 s[92:93], s[92:93], vcc\n"
    "v_pk_mul_f32 v[130:131], v[130:131], v[20:21] op_sel:[0,1] op_sel_hi:[1,1]\n"
    "v_cmp_nge_f32 vcc, s96, v60\n"
    "v_pk_add_f32 v[132:133], v[132:133], v[252:253]\n"
    "v_pk_add_f32 v[8:9], v[8:9], v[12:13]\n"
    "v_pk_add_f32 v[134:135], v[134:135], v[254:255]\n"
    "v_pk_add_f32 v[10:11], v[10:11], v[14:15]\n"
    "v_pk_add_f32 v[132:133], v[132:133], v[8:9]\n"
    "s_or_b64 s[92:93], s[92:93], vcc\n"
    "v_pk_add_f32 v[134:135], v[134:135], v[10:11]\n"
    "v_pk_mul_f32 v[132:133], v[132:133], v[20:21] op_sel:[0,1] op_sel_hi:[1,1]\n"
    "v_pk_mul_f32 v[134:135], v[134:135], v[20:21] op_sel:[0,1] op_sel_hi:[1,1]\n"
    "v_mfma_f32_16x16x32_bf16 v[48:51], v[196:199], a[36:39], v[48:51]\n"
    "v_cvt_pk_bf16_f32 v55, v42, v43\n"
    "v_add_f32 v24, v24, v43\n"
    "v_cvt_pk_bf16_f32 v240, v128, v129\n"
    "v_cvt_pk_bf16_f32 v241, v130, v131\n"
    "v_mfma_f32_16x16x32_bf16 v[48:51], v[200:203], a[40:43], v[48:51]\n"
    "ds_write_b128 v16, a[128:131] offset:8192\n"
    "v_exp_f32 v44, v44\n"
    "v_cvt_pk_bf16_f32 v242, v132, v133\n"
    "v_cvt_pk_bf16_f32 v243, v134, v135\n"
    "v_mfma_f32_16x16x32_bf16 v[48:51], v[204:207], a[44:47], v[48:51]\n"
    "v_add_f32 v24, v24, v44\n"
    "v_permlane16_swap_b32 v240, v242\n"
    "v_permlane16_swap_b32 v241, v243\n"
    "v_mfma_f32_16x16x32_bf16 v[144:147], v[96:99], v[52:55], v[144:147]\n"
    "v_exp_f32 v45, v45\n"
    "buffer_store_dwordx4 v[240:243], v20, s[52:55], 0 offen nt\n"
    "v_mfma_f32_16x16x32_bf16 a[244:247], a[240:243], v[240:243], 0\n"
    "v_mfma_f32_16x16x32_bf16 v[148:151], v[100:103], v[52:55], v[148:151]\n"
    "v_cvt_pk_bf16_f32 v56, v44, v45\n"
    "v_add_f32 v24, v24, v45\n"
    "v_mfma_f32_16x16x32_bf16 a[144:147], v[104:107], v[52:55], a[144:147]\n"
    "ds_write_b128 v16, a[132:135] offset:9216\n"
    "v_exp_f32 v46, v46\n"
    "v_mfma_f32_16x16x32_bf16 a[148:151], v[108:111], v[52:55], a[148:151]\n"
    "v_add_f32 v24, v24, v46\n"
    "v_mfma_f32_16x16x32_bf16 a[152:155], v[112:115], v[52:55], a[152:155]\n"
    "v_exp_f32 v47, v47\n"
    "v_mfma_f32_16x16x32_bf16 a[156:159], v[116:119], v[52:55], a[156:159]\n"
    "v_cvt_pk_bf16_f32 v57, v46, v47\n"
    "v_add_f32 v24, v24, v47\n"
    "v_mfma_f32_16x16x32_bf16 a[160:163], v[120:123], v[52:55], a[160:163]\n"
    "ds_write_b128 v16, a[136:139] offset:10240\n"
    "v_exp_f32 v48, v48\n"
    "v_mfma_f32_16x16x32_bf16 a[164:167], v[124:127], v[52:55], a[164:167]\n"
    "v_add_f32 v24, v24, v48\n"
    "v_mfma_f32_16x16x32_bf16 v[36:39], v[64:67], a[48:51], 0\n"
    "v_exp_f32 v49, v49\n"
    "v_mfma_f32_16x16x32_bf16 v[36:39], v[68:71], a[52:55], v[36:39]\n"
    "v_cvt_pk_bf16_f32 v58, v48, v49\n"
    "v_add_f32 v24, v24, v49\n"
    "v_mfma_f32_16x16x32_bf16 v[36:39], v[72:75], a[56:59], v[36:39]\n"
    "ds_write_b128 v16, a[140:143] offset:11264\n"
    "v_exp_f32 v50, v50\n"
    "v_mfma_f32_16x16x32_bf16 v[36:39], v[76:79], a[60:63], v[36:39]\n"
    "v_add_f32 v24, v24, v50\n"
    "v_mfma_f32_16x16x32_bf16 v[40:43], v[80:83], a[48:51], 0\n"
    "v_exp_f32 v51, v51\n"
    "v_mfma_f32_16x16x32_bf16 v[40:43], v[84:87], a[52:55], v[40:43]\n"
    "v_cvt_pk_bf16_f32 v59, v50, v51\n"
    "v_add_f32 v24, v24, v51\n"
    "s_waitcnt lgkmcnt(0)\n"
    "s_barrier\n"
    "v_mfma_f32_16x16x32_bf16 v[40:43], v[88:91], a[56:59], v[40:43]\n"
    "ds_read_b128 v[60:63], v19 offset:256\n"
    "v_exp_f32 v36, v36\n"
    "v_mov_b32 v28, v24\n"
    "v_mfma_f32_16x16x32_bf16 v[40:43], v[92:95], a[60:63], v[40:43]\n"
    "ds_read_b128 v[240:243], v4 offset:6144\n"
    "v_add_f32 v25, v25, v36\n"
    "v_permlane32_swap_b32 v28, v24\n"
    "v_mfma_f32_16x16x32_bf16 a[144:147], v[216:219], v[56:59], a[144:147]\n"
    "ds_read_b128 v[244:247], v5 offset:8192\n"
    "v_exp_f32 v37, v37\n"
    "v_add_f32 v24, v24, v28\n"
    "v_mfma_f32_16x16x32_bf16 a[148:151], v[220:223], v[56:59], a[148:151]\n"
    "ds_read_b128 v[248:251], v6 offset:10240\n"
    "v_cvt_pk_bf16_f32 v52, v36, v37\n"
    "v_add_f32 v25, v25, v37\n"
    "v_mov_b32 v28, v24\n"
    "v_mfma_f32_16x16x32_bf16 a[152:155], v[224:227], v[56:59], a[152:155]\n"
    "ds_read_b128 v[252:255], v4 offset:7168\n"
    "v_exp_f32 v38, v38\n"
    "v_permlane16_swap_b32 v28, v24\n"
    "v_mfma_f32_16x16x32_bf16 a[156:159], v[228:231], v[56:59], a[156:159]\n"
    "ds_read_b128 v[8:11], v5 offset:9216\n"
    "v_add_f32 v25, v25, v38\n"
    "v_add_f32 v24, v24, v28\n"
    "v_mfma_f32_16x16x32_bf16 a[160:163], v[232:235], v[56:59], a[160:163]\n"
    "ds_read_b128 v[12:15], v6 offset:11264\n"
    "v_exp_f32 v39, v39\n"
    "ds_write_b32 v18, v24 offset:512\n"
    "v_mfma_f32_16x16x32_bf16 a[164:167], v[236:239], v[56:59], a[164:167]\n"
    "v_cvt_pk_bf16_f32 v53, v38, v39\n"
    "v_add_f32 v25, v25, v39\n"
    "s_waitcnt lgkmcnt(1)\n"
    "v_mfma_f32_16x16x32_bf16 v[144:147], v[208:211], v[56:59], v[144:147]\n"
    "ds_write_b128 v16, a[144:147] offset:12288\n"
    "v_exp_f32 v40, v40\n"
    "v_add_f32 v60, v60, v61\n"
    "v_mfma_f32_16x16x32_bf16 v[148:151], v[212:215], v[56:59], v[148:151]\n"
    "v_add_f32 v25, v25, v40\n"
    "v_add_f32 v62, v62, v63\n"
    "v_mfma_f32_16x16x32_bf16 v[44:47], v[176:179], a[48:51], 0\n"
    "v_exp_f32 v41, v41\n"
    "v_add_f32 v60, v60, v62\n"
    "v_mfma_f32_16x16x32_bf16 v[44:47], v[180:183], a[52:55], v[44:47]\n"
    "v_cvt_pk_bf16_f32 v54, v40, v41\n"
    "v_add_f32 v25, v25, v41\n"
    "v_rcp_f32 v21, v60\n"
    "v_mfma_f32_16x16x32_bf16 v[44:47], v[184:187], a[56:59], v[44:47]\n"
    "ds_write_b128 v16, a[148:151] offset:13312\n"
    "v_exp_f32 v42, v42\n"
    "v_mfma_f32_16x16x32_bf16 v[44:47], v[188:191], a[60:63], v[44:47]\n"
    "v_add_f32 v25, v25, v42\n"
    "v_mfma_f32_16x16x32_bf16 v[48:51], v[192:195], a[48:51], 0\n"
    "v_exp_f32 v43, v43\n"
    "v_pk_add_f32 v[136:137], v[136:137], v[240:241]\n"
    "v_pk_add_f32 v[244:245], v[244:245], v[248:249]\n"
    "v_cmp_nle_f32 vcc, s95, v60\n"
    "v_pk_add_f32 v[138:139], v[138:139], v[242:243]\n"
    "v_pk_add_f32 v[246:247], v[246:247], v[250:251]\n"
    "v_pk_add_f32 v[136:137], v[136:137], v[244:245]\n"
    "v_pk_add_f32 v[138:139], v[138:139], v[246:247]\n"
    "v_pk_mul_f32 v[136:137], v[136:137], v[20:21] op_sel:[0,1] op_sel_hi:[1,1]\n"
    "s_or_b64 s[92:93], s[92:93], vcc\n"
    "v_pk_mul_f32 v[138:139], v[138:139], v[20:21] op_sel:[0,1] op_sel_hi:[1,1]\n"
    "v_cmp_nge_f32 vcc, s96, v60\n"
    "v_pk_add_f32 v[140:141], v[140:141], v[252:253]\n"
    "v_pk_add_f32 v[8:9], v[8:9], v[12:13]\n"
    "v_pk_add_f32 v[142:143], v[142:143], v[254:255]\n"
    "v_pk_add_f32 v[10:11], v[10:11], v[14:15]\n"
    "v_pk_add_f32 v[140:141], v[140:141], v[8:9]\n"
    "s_or_b64 s[92:93], s[92:93], vcc\n"
    "v_pk_add_f32 v[142:143], v[142:143], v[10:11]\n"
    "v_pk_mul_f32 v[140:141], v[140:141], v[20:21] op_sel:[0,1] op_sel_hi:[1,1]\n"
    "v_pk_mul_f32 v[142:143], v[142:143], v[20:21] op_sel:[0,1] op_sel_hi:[1,1]\n"
    "v_mfma_f32_16x16x32_bf16 v[48:51], v[196:199], a[52:55], v[48:51]\n"
    "v_cvt_pk_bf16_f32 v55, v42, v43\n"
    "v_add_f32 v25, v25, v43\n"
    "v_cvt_pk_bf16_f32 v240, v136, v137\n"
    "v_cvt_pk_bf16_f32 v241, v138, v139\n"
    "v_mfma_f32_16x16x32_bf16 v[48:51], v[200:203], a[56:59], v[48:51]\n"
    "ds_write_b128 v16, a[152:155] offset:14336\n"
    "v_exp_f32 v44, v44\n"
    "v_cvt_pk_bf16_f32 v242, v140, v141\n"
    "v_cvt_pk_bf16_f32 v243, v142, v143\n"
    "v_mfma_f32_16x16x32_bf16 v[48:51], v[204:207], a[60:63], v[48:51]\n"
    "v_add_f32 v25, v25, v44\n"
    "v_permlane16_swap_b32 v240, v242\n"
    "v_permlane16_swap_b32 v241, v243\n"
    "v_mfma_f32_16x16x32_bf16 v[152:155], v[96:99], v[52:55], v[152:155]\n"
    "v_exp_f32 v45, v45\n"
    "buffer_store_dwordx4 v[240:243], v20, s[52:55], s57 offen nt\n"
    "v_mfma_f32_16x16x32_bf16 a[244:247], a[240:243], v[240:243], a[244:247]\n"
    "v_mfma_f32_16x16x32_bf16 v[156:159], v[100:103], v[52:55], v[156:159]\n"
    "v_cvt_pk_bf16_f32 v56, v44, v45\n"
    "v_add_f32 v25, v25, v45\n"
    "v_mfma_f32_16x16x32_bf16 a[168:171], v[104:107], v[52:55], a[168:171]\n"
    "ds_write_b128 v16, a[156:159] offset:15360\n"
    "v_exp_f32 v46, v46\n"
    "v_mfma_f32_16x16x32_bf16 a[172:175], v[108:111], v[52:55], a[172:175]\n"
    "v_add_f32 v25, v25, v46\n"
    "v_mfma_f32_16x16x32_bf16 a[176:179], v[112:115], v[52:55], a[176:179]\n"
    "v_exp_f32 v47, v47\n"
    "v_mfma_f32_16x16x32_bf16 a[180:183], v[116:119], v[52:55], a[180:183]\n"
    "v_cvt_pk_bf16_f32 v57, v46, v47\n"
    "v_add_f32 v25, v25, v47\n"
    "v_mfma_f32_16x16x32_bf16 a[184:187], v[120:123], v[52:55], a[184:187]\n"
    "ds_write_b128 v16, a[160:163] offset:16384\n"
    "v_exp_f32 v48, v48\n"
    "v_mfma_f32_16x16x32_bf16 a[188:191], v[124:127], v[52:55], a[188:191]\n"
    "v_add_f32 v25, v25, v48\n"
    "v_mfma_f32_16x16x32_bf16 v[36:39], v[64:67], a[64:67], 0\n"
    "v_exp_f32 v49, v49\n"
    "v_mfma_f32_16x16x32_bf16 v[36:39], v[68:71], a[68:71], v[36:39]\n"
    "v_cvt_pk_bf16_f32 v58, v48, v49\n"
    "v_add_f32 v25, v25, v49\n"
    "v_mfma_f32_16x16x32_bf16 v[36:39], v[72:75], a[72:75], v[36:39]\n"
    "ds_write_b128 v16, a[164:167] offset:17408\n"
    "v_exp_f32 v50, v50\n"
    "v_mfma_f32_16x16x32_bf16 v[36:39], v[76:79], a[76:79], v[36:39]\n"
    "v_add_f32 v25, v25, v50\n"
    "v_mfma_f32_16x16x32_bf16 v[40:43], v[80:83], a[64:67], 0\n"
    "v_exp_f32 v51, v51\n"
    "v_mfma_f32_16x16x32_bf16 v[40:43], v[84:87], a[68:71], v[40:43]\n"
    "v_cvt_pk_bf16_f32 v59, v50, v51\n"
    "v_add_f32 v25, v25, v51\n"
    "s_waitcnt lgkmcnt(0)\n"
    "s_barrier\n"
    "v_mfma_f32_16x16x32_bf16 v[40:43], v[88:91], a[72:75], v[40:43]\n"
    "ds_read_b128 v[60:63], v19 offset:512\n"
    "v_exp_f32 v36, v36\n"
    "v_mov_b32 v28, v25\n"
    "v_mfma_f32_16x16x32_bf16 v[40:43], v[92:95], a[76:79], v[40:43]\n"
    "ds_read_b128 v[240:243], v4 offset:12288\n"
    "v_add_f32 v26, v26, v36\n"
    "v_permlane32_swap_b32 v28, v25\n"
    "v_mfma_f32_16x16x32_bf16 a[168:171], v[216:219], v[56:59], a[168:171]\n"
    "ds_read_b128 v[244:247], v5 offset:14336\n"
    "v_exp_f32 v37, v37\n"
    "v_add_f32 v25, v25, v28\n"
    "v_mfma_f32_16x16x32_bf16 a[172:175], v[220:223], v[56:59], a[172:175]\n"
    "ds_read_b128 v[248:251], v6 offset:16384\n"
    "v_cvt_pk_bf16_f32 v52, v36, v37\n"
    "v_add_f32 v26, v26, v37\n"
    "v_mov_b32 v28, v25\n"
    "v_mfma_f32_16x16x32_bf16 a[176:179], v[224:227], v[56:59], a[176:179]\n"
    "ds_read_b128 v[252:255], v4 offset:13312\n"
    "v_exp_f32 v38, v38\n"
    "v_permlane16_swap_b32 v28, v25\n"
    "v_mfma_f32_16x16x32_bf16 a[180:183], v[228:231], v[56:59], a[180:183]\n"
    "ds_read_b128 v[8:11], v5 offset:15360\n"
    "v_add_f32 v26, v26, v38\n"
    "v_add_f32 v25, v25, v28\n"
    "v_mfma_f32_16x16x32_bf16 a[184:187], v[232:235], v[56:59], a[184:187]\n"
    "ds_read_b128 v[12:15], v6 offset:17408\n"
    "v_exp_f32 v39, v39\n"
    "ds_write_b32 v18, v25 offset:768\n"
    "v_mfma_f32_16x16x32_bf16 a[188:191], v[236:239], v[56:59], a[188:191]\n"
    "v_cvt_pk_bf16_f32 v53, v38, v39\n"
    "v_add_f32 v26, v26, v39\n"
    "s_waitcnt lgkmcnt(1)\n"
    "v_mfma_f32_16x16x32_bf16 v[152:155], v[208:211], v[56:59], v[152:155]\n"
    "ds_write_b128 v16, a[168:171] offset:18432\n"
    "v_exp_f32 v40, v40\n"
    "v_add_f32 v60, v60, v61\n"
    "v_mfma_f32_16x16x32_bf16 v[156:159], v[212:215], v[56:59], v[156:159]\n"
    "v_add_f32 v26, v26, v40\n"
    "v_add_f32 v62, v62, v63\n"
    "v_mfma_f32_16x16x32_bf16 v[44:47], v[176:179], a[64:67], 0\n"
    "v_exp_f32 v41, v41\n"
    "v_add_f32 v60, v60, v62\n"
    "v_mfma_f32_16x16x32_bf16 v[44:47], v[180:183], a[68:71], v[44:47]\n"
    "v_cvt_pk_bf16_f32 v54, v40, v41\n"
    "v_add_f32 v26, v26, v41\n"
    "v_rcp_f32 v21, v60\n"
    "v_mfma_f32_16x16x32_bf16 v[44:47], v[184:187], a[72:75], v[44:47]\n"
    "ds_write_b128 v16, a[172:175] offset:19456\n"
    "v_exp_f32 v42, v42\n"
    "v_mfma_f32_16x16x32_bf16 v[44:47], v[188:191], a[76:79], v[44:47]\n"
    "v_add_f32 v26, v26, v42\n"
    "v_mfma_f32_16x16x32_bf16 v[48:51], v[192:195], a[64:67], 0\n"
    "v_exp_f32 v43, v43\n"
    "v_pk_add_f32 v[144:145], v[144:145], v[240:241]\n"
    "v_pk_add_f32 v[244:245], v[244:245], v[248:249]\n"
    "v_cmp_nle_f32 vcc, s95, v60\n"
    "v_pk_add_f32 v[146:147], v[146:147], v[242:243]\n"
    "v_pk_add_f32 v[246:247], v[246:247], v[250:251]\n"
    "v_pk_add_f32 v[144:145], v[144:145], v[244:245]\n"
    "v_pk_add_f32 v[146:147], v[146:147], v[246:247]\n"
    "v_pk_mul_f32 v[144:145], v[144:145], v[20:21] op_sel:[0,1] op_sel_hi:[1,1]\n"
    "s_or_b64 s[92:93], s[92:93], vcc\n"
    "v_pk_mul_f32 v[146:147], v[146:147], v[20:21] op_sel:[0,1] op_sel_hi:[1,1]\n"
    "v_cmp_nge_f32 vcc, s96, v60\n"
    "v_pk_add_f32 v[148:149], v[148:149], v[252:253]\n"
    "v_pk_add_f32 v[8:9], v[8:9], v[12:13]\n"
    "v_pk_add_f32 v[150:151], v[150:151], v[254:255]\n"
    "v_pk_add_f32 v[10:11], v[10:11], v[14:15]\n"
    "v_pk_add_f32 v[148:149], v[148:149], v[8:9]\n"
    "s_or_b64 s[92:93], s[92:93], vcc\n"
    "v_pk_add_f32 v[150:151], v[150:151], v[10:11]\n"
    "v_pk_mul_f32 v[148:149], v[148:149], v[20:21] op_sel:[0,1] op_sel_hi:[1,1]\n"
    "v_pk_mul_f32 v[150:151], v[150:151], v[20:21] op_sel:[0,1] op_sel_hi:[1,1]\n"
    "v_mfma_f32_16x16x32_bf16 v[48:51], v[196:199], a[68:71], v[48:51]\n"
    "v_cvt_pk_bf16_f32 v55, v42, v43\n"
    "v_add_f32 v26, v26, v43\n"
    "v_cvt_pk_bf16_f32 v240, v144, v145\n"
    "v_cvt_pk_bf16_f32 v241, v146, v147\n"
    "v_mfma_f32_16x16x32_bf16 v[48:51], v[200:203], a[72:75], v[48:51]\n"
    "ds_write_b128 v16, a[176:179] offset:20480\n"
    "v_exp_f32 v44, v44\n"
    "v_cvt_pk_bf16_f32 v242, v148, v149\n"
    "v_cvt_pk_bf16_f32 v243, v150, v151\n"
    "v_mfma_f32_16x16x32_bf16 v[48:51], v[204:207], a[76:79], v[48:51]\n"
    "v_add_f32 v26, v26, v44\n"
    "v_permlane16_swap_b32 v240, v242\n"
    "v_permlane16_swap_b32 v241, v243\n"
    "v_mfma_f32_16x16x32_bf16 v[160:163], v[96:99], v[52:55], v[160:163]\n"
    "v_exp_f32 v45, v45\n"
    "buffer_store_dwordx4 v[240:243], v20, s[52:55], s58 offen nt\n"
    "v_mfma_f32_16x16x32_bf16 a[244:247], a[240:243], v[240:243], a[244:247]\n"
    "v_mfma_f32_16x16x32_bf16 v[164:167], v[100:103], v[52:55], v[164:167]\n"
    "v_cvt_pk_bf16_f32 v56, v44, v45\n"
    "v_add_f32 v26, v26, v45\n"
    "v_mfma_f32_16x16x32_bf16 a[192:195], v[104:107], v[52:55], a[192:195]\n"
    "ds_write_b128 v16, a[180:183] offset:21504\n"
    "v_exp_f32 v46, v46\n"
    "v_mfma_f32_16x16x32_bf16 a[196:199], v[108:111], v[52:55], a[196:199]\n"
    "v_add_f32 v26, v26, v46\n"
    "v_mfma_f32_16x16x32_bf16 a[200:203], v[112:115], v[52:55], a[200:203]\n"
    "v_exp_f32 v47, v47\n"
    "v_mfma_f32_16x16x32_bf16 a[204:207], v[116:119], v[52:55], a[204:207]\n"
    "v_cvt_pk_bf16_f32 v57, v46, v47\n"
    "v_add_f32 v26, v26, v47\n"
    "v_mfma_f32_16x16x32_bf16 a[208:211], v[120:123], v[52:55], a[208:211]\n"
    "ds_write_b128 v16, a[184:187] offset:22528\n"
    "v_exp_f32 v48, v48\n"
    "v_mfma_f32_16x16x32_bf16 a[212:215], v[124:127], v[52:55], a[212:215]\n"
    "v_add_f32 v26, v26, v48\n"
    "v_mfma_f32_16x16x32_bf16 v[36:39], v[64:67], a[80:83], 0\n"
    "v_exp_f32 v49, v49\n"
    "v_mfma_f32_16x16x32_bf16 v[36:39], v[68:71], a[84:87], v[36:39]\n"
    "v_cvt_pk_bf16_f32 v58, v48, v49\n"
    "v_add_f32 v26, v26, v49\n"
    "v_mfma_f32_16x16x32_bf16 v[36:39], v[72:75], a[88:91], v[36:39]\n"
    "ds_write_b128 v16, a[188:191] offset:23552\n"
    "v_exp_f32 v50, v50\n"
    "v_mfma_f32_16x16x32_bf16 v[36:39], v[76:79], a[92:95], v[36:39]\n"
    "v_add_f32 v26, v26, v50\n"
    "v_mfma_f32_16x16x32_bf16 v[40:43], v[80:83], a[80:83], 0\n"
    "v_exp_f32 v51, v51\n"
    "v_mfma_f32_16x16x32_bf16 v[40:43], v[84:87], a[84:87], v[40:43]\n"
    "v_cvt_pk_bf16_f32 v59, v50, v51\n"
    "v_add_f32 v26, v26, v51\n"
    "s_waitcnt lgkmcnt(0)\n"
    "s_barrier\n"
    "v_mfma_f32_16x16x32_bf16 v[40:43], v[88:91], a[88:91], v[40:43]\n"
    "ds_read_b128 v[60:63], v19 offset:768\n"
    "v_exp_f32 v36, v36\n"
    "v_mov_b32 v28, v26\n"
    "v_mfma_f32_16x16x32_bf16 v[40:43], v[92:95], a[92:95], v[40:43]\n"
    "ds_read_b128 v[240:243], v4 offset:18432\n"
    "v_add_f32 v27, v27, v36\n"
    "v_permlane32_swap_b32 v28, v26\n"
    "v_mfma_f32_16x16x32_bf16 a[192:195], v[216:219], v[56:59], a[192:195]\n"
    "ds_read_b128 v[244:247], v5 offset:20480\n"
    "v_exp_f32 v37, v37\n"
    "v_add_f32 v26, v26, v28\n"
    "v_mfma_f32_16x16x32_bf16 a[196:199], v[220:223], v[56:59], a[196:199]\n"
    "ds_read_b128 v[248:251], v6 offset:22528\n"
    "v_cvt_pk_bf16_f32 v52, v36, v37\n"
    "v_add_f32 v27, v27, v37\n"
    "v_mov_b32 v28, v26\n"
    "v_mfma_f32_16x16x32_bf16 a[200:203], v[224:227], v[56:59], a[200:203]\n"
    "ds_read_b128 v[252:255], v4 offset:19456\n"
    "v_exp_f32 v38, v38\n"
    "v_permlane16_swap_b32 v28, v26\n"
    "v_mfma_f32_16x16x32_bf16 a[204:207], v[228:231], v[56:59], a[204:207]\n"
    "ds_read_b128 v[8:11], v5 offset:21504\n"
    "v_add_f32 v27, v27, v38\n"
    "v_add_f32 v26, v26, v28\n"
    "v_mfma_f32_16x16x32_bf16 a[208:211], v[232:235], v[56:59], a[208:211]\n"
    "ds_read_b128 v[12:15], v6 offset:23552\n"
    "v_exp_f32 v39, v39\n"
    "ds_write_b32 v18, v26 offset:1024\n"
    "v_mfma_f32_16x16x32_bf16 a[212:215], v[236:239], v[56:59], a[212:215]\n"
    "v_cvt_pk_bf16_f32 v53, v38, v39\n"
    "v_add_f32 v27, v27, v39\n"
    "s_waitcnt lgkmcnt(1)\n"
    "v_mfma_f32_16x16x32_bf16 v[160:163], v[208:211], v[56:59], v[160:163]\n"
    "ds_write_b128 v16, a[192:195] offset:24576\n"
    "v_exp_f32 v40, v40\n"
    "v_add_f32 v60, v60, v61\n"
    "v_mfma_f32_16x16x32_bf16 v[164:167], v[212:215], v[56:59], v[164:167]\n"
    "v_add_f32 v27, v27, v40\n"
    "v_add_f32 v62, v62, v63\n"
    "v_mfma_f32_16x16x32_bf16 v[44:47], v[176:179], a[80:83], 0\n"
    "v_exp_f32 v41, v41\n"
    "v_add_f32 v60, v60, v62\n"
    "v_mfma_f32_16x16x32_bf16 v[44:47], v[180:183], a[84:87], v[44:47]\n"
    "v_cvt_pk_bf16_f32 v54, v40, v41\n"
    "v_add_f32 v27, v27, v41\n"
    "v_rcp_f32 v21, v60\n"
    "v_mfma_f32_16x16x32_bf16 v[44:47], v[184:187], a[88:91], v[44:47]\n"
    "ds_write_b128 v16, a[196:199] offset:25600\n"
    "v_exp_f32 v42, v42\n"
    "v_mfma_f32_16x16x32_bf16 v[44:47], v[188:191], a[92:95], v[44:47]\n"
    "v_add_f32 v27, v27, v42\n"
    "v_mfma_f32_16x16x32_bf16 v[48:51], v[192:195], a[80:83], 0\n"
    "v_exp_f32 v43, v43\n"
    "v_pk_add_f32 v[152:153], v[152:153], v[240:241]\n"
    "v_pk_add_f32 v[244:245], v[244:245], v[248:249]\n"
    "v_cmp_nle_f32 vcc, s95, v60\n"
    "v_pk_add_f32 v[154:155], v[154:155], v[242:243]\n"
    "v_pk_add_f32 v[246:247], v[246:247], v[250:251]\n"
    "v_pk_add_f32 v[152:153], v[152:153], v[244:245]\n"
    "v_pk_add_f32 v[154:155], v[154:155], v[246:247]\n"
    "v_pk_mul_f32 v[152:153], v[152:153], v[20:21] op_sel:[0,1] op_sel_hi:[1,1]\n"
    "s_or_b64 s[92:93], s[92:93], vcc\n"
    "v_pk_mul_f32 v[154:155], v[154:155], v[20:21] op_sel:[0,1] op_sel_hi:[1,1]\n"
    "v_cmp_nge_f32 vcc, s96, v60\n"
    "v_pk_add_f32 v[156:157], v[156:157], v[252:253]\n"
    "v_pk_add_f32 v[8:9], v[8:9], v[12:13]\n"
    "v_pk_add_f32 v[158:159], v[158:159], v[254:255]\n"
    "v_pk_add_f32 v[10:11], v[10:11], v[14:15]\n"
    "v_pk_add_f32 v[156:157], v[156:157], v[8:9]\n"
    "s_or_b64 s[92:93], s[92:93], vcc\n"
    "v_pk_add_f32 v[158:159], v[158:159], v[10:11]\n"
    "v_pk_mul_f32 v[156:157], v[156:157], v[20:21] op_sel:[0,1] op_sel_hi:[1,1]\n"
    "v_pk_mul_f32 v[158:159], v[158:159], v[20:21] op_sel:[0,1] op_sel_hi:[1,1]\n"
    "v_mfma_f32_16x16x32_bf16 v[48:51], v[196:199], a[84:87], v[48:51]\n"
    "v_cvt_pk_bf16_f32 v55, v42, v43\n"
    "v_add_f32 v27, v27, v43\n"
    "v_cvt_pk_bf16_f32 v240, v152, v153\n"
    "v_cvt_pk_bf16_f32 v241, v154, v155\n"
    "v_mfma_f32_16x16x32_bf16 v[48:51], v[200:203], a[88:91], v[48:51]\n"
    "ds_write_b128 v16, a[200:203] offset:26624\n"
    "v_exp_f32 v44, v44\n"
    "v_cvt_pk_bf16_f32 v242, v156, v157\n"
    "v_cvt_pk_bf16_f32 v243, v158, v159\n"
    "v_mfma_f32_16x16x32_bf16 v[48:51], v[204:207], a[92:95], v[48:51]\n"
    "v_exp_f32 v45, v45\n"
    "v_add_f32 v27, v27, v44\n"
    "v_permlane16_swap_b32 v240, v242\n"
    "v_permlane16_swap_b32 v241, v243\n"
    "v_mfma_f32_16x16x32_bf16 v[168:171], v[96:99], v[52:55], v[168:171]\n"
    "v_exp_f32 v46, v46\n"
    "v_cvt_pk_bf16_f32 v56, v44, v45\n"
    "v_add_f32 v27, v27, v45\n"
    "buffer_store_dwordx4 v[240:243], v20, s[52:55], s59 offen nt\n"
    "v_mfma_f32_16x16x32_bf16 a[244:247], a[240:243], v[240:243], a[244:247]\n"
    "v_mfma_f32_16x16x32_bf16 v[172:175], v[100:103], v[52:55], v[172:175]\n"
    "v_exp_f32 v47, v47\n"
    "v_add_f32 v27, v27, v46\n"
    "v_mfma_f32_16x16x32_bf16 a[216:219], v[104:107], v[52:55], a[216:219]\n"
    "ds_write_b128 v16, a[204:207] offset:27648\n"
    "v_cvt_pk_bf16_f32 v57, v46, v47\n"
    "v_add_f32 v27, v27, v47\n"
    "v_mfma_f32_16x16x32_bf16 a[220:223], v[108:111], v[52:55], a[220:223]\n"
    "v_exp_f32 v48, v48\n"
    "v_mfma_f32_16x16x32_bf16 a[224:227], v[112:115], v[52:55], a[224:227]\n"
    "v_exp_f32 v49, v49\n"
    "v_add_f32 v27, v27, v48\n"
    "v_mfma_f32_16x16x32_bf16 a[228:231], v[116:119], v[52:55], a[228:231]\n"
    "v_exp_f32 v50, v50\n"
    "v_cvt_pk_bf16_f32 v58, v48, v49\n"
    "v_add_f32 v27, v27, v49\n"
    "v_mfma_f32_16x16x32_bf16 a[232:235], v[120:123], v[52:55], a[232:235]\n"
    "ds_write_b128 v16, a[208:211] offset:28672\n"
    "v_exp_f32 v51, v51\n"
    "v_add_f32 v27, v27, v50\n"
    "v_mfma_f32_16x16x32_bf16 a[236:239], v[124:127], v[52:55], a[236:239]\n"
    "v_cvt_pk_bf16_f32 v59, v50, v51\n"
    "v_add_f32 v27, v27, v51\n"
    "s_nop 0\n"
    "v_mfma_f32_16x16x32_bf16 a[216:219], v[216:219], v[56:59], a[216:219]\n"
    "v_mov_b32 v28, v27\n"
    "v_mfma_f32_16x16x32_bf16 a[220:223], v[220:223], v[56:59], a[220:223]\n"
    "s_nop 0\n"
    "v_permlane32_swap_b32 v28, v27\n"
    "v_mfma_f32_16x16x32_bf16 a[224:227], v[224:227], v[56:59], a[224:227]\n"
    "ds_write_b128 v16, a[212:215] offset:29696\n"
    "v_add_f32 v27, v27, v28\n"
    "v_mfma_f32_16x16x32_bf16 a[228:231], v[228:231], v[56:59], a[228:231]\n"
    "v_mov_b32 v28, v27\n"
    "v_mfma_f32_16x16x32_bf16 a[232:235], v[232:235], v[56:59], a[232:235]\n"
    "s_nop 0\n"
    "v_permlane16_swap_b32 v28, v27\n"
    "v_mfma_f32_16x16x32_bf16 a[236:239], v[236:239], v[56:59], a[236:239]\n"
    "v_add_f32 v27, v27, v28\n"
    "s_waitcnt lgkmcnt(0)\n"
    "s_barrier\n"
    "v_mfma_f32_16x16x32_bf16 v[168:171], v[208:211], v[56:59], v[168:171]\n"
    "ds_read_b128 v[60:63], v19 offset:1024\n"
    "ds_write_b32 v18, v27 offset:1280\n"
    "v_mfma_f32_16x16x32_bf16 v[172:175], v[212:215], v[56:59], v[172:175]\n"
    "ds_read_b128 v[240:243], v4 offset:24576\n"
    "ds_write_b128 v16, a[216:219] offset:30720\n"
    "ds_read_b128 v[244:247], v5 offset:26624\n"
    "ds_read_b128 v[248:251], v6 offset:28672\n"
    "ds_read_b128 v[252:255], v4 offset:25600\n"
    "ds_read_b128 v[8:11], v5 offset:27648\n"
    "ds_read_b128 v[12:15], v6 offset:29696\n"
    "ds_write_b128 v16, a[220:223] offset:31744\n"
    "s_waitcnt lgkmcnt(9)\n"
    "v_add_f32 v60, v60, v61\n"
    "v_add_f32 v62, v62, v63\n"
    "v_add_f32 v60, v60, v62\n"
    "v_rcp_f32 v21, v60\n"
    "s_waitcnt lgkmcnt(7)\n"
    "v_pk_add_f32 v[160:161], v[160:161], v[240:241]\n"
    "s_waitcnt lgkmcnt(4)\n"
    "v_pk_add_f32 v[244:245], v[244:245], v[248:249]\n"
    "v_cmp_nle_f32 vcc, s95, v60\n"
    "ds_write_b128 v17, a[224:227] offset:0\n"
    "v_pk_add_f32 v[162:163], v[162:163], v[242:243]\n"
    "v_pk_add_f32 v[246:247], v[246:247], v[250:251]\n"
    "v_pk_add_f32 v[160:161], v[160:161], v[244:245]\n"
    "v_pk_add_f32 v[162:163], v[162:163], v[246:247]\n"
    "v_pk_mul_f32 v[160:161], v[160:161], v[20:21] op_sel:[0,1] op_sel_hi:[1,1]\n"
    "s_or_b64 s[92:93], s[92:93], vcc\n"
    "v_pk_mul_f32 v[162:163], v[162:163], v[20:21] op_sel:[0,1] op_sel_hi:[1,1]\n"
    "ds_write_b128 v17, a[228:231] offset:1024\n"
    "v_cmp_nge_f32 vcc, s96, v60\n"
    "s_waitcnt lgkmcnt(5)\n"
    "v_pk_add_f32 v[164:165], v[164:165], v[252:253]\n"
    "s_waitcnt lgkmcnt(3)\n"
    "v_pk_add_f32 v[8:9], v[8:9], v[12:13]\n"
    "v_pk_add_f32 v[166:167], v[166:167], v[254:255]\n"
    "v_pk_add_f32 v[10:11], v[10:11], v[14:15]\n"
    "v_pk_add_f32 v[164:165], v[164:165], v[8:9]\n"
    "s_or_b64 s[92:93], s[92:93], vcc\n"
    "ds_write_b128 v17, a[232:235] offset:2048\n"
    "v_pk_add_f32 v[166:167], v[166:167], v[10:11]\n"
    "v_pk_mul_f32 v[164:165], v[164:165], v[20:21] op_sel:[0,1] op_sel_hi:[1,1]\n"
    "v_pk_mul_f32 v[166:167], v[166:167], v[20:21] op_sel:[0,1] op_sel_hi:[1,1]\n"
    "v_cvt_pk_bf16_f32 v240, v160, v161\n"
    "v_cvt_pk_bf16_f32 v241, v162, v163\n"
    "v_cvt_pk_bf16_f32 v242, v164, v165\n"
    "v_cvt_pk_bf16_f32 v243, v166, v167\n"
    "ds_write_b128 v17, a[236:239] offset:3072\n"
    "v_permlane16_swap_b32 v240, v242\n"
    "v_permlane16_swap_b32 v241, v243\n"
    "buffer_store_dwordx4 v[240:243], v20, s[52:55], s60 offen nt\n"
    "s_nop 0\n"
    "v_mfma_f32_16x16x32_bf16 a[244:247], a[240:243], v[240:243], a[244:247]\n"
    "s_waitcnt lgkmcnt(0)\n"
    "s_barrier\n"
    "ds_read_b128 v[88:91], v19 offset:1280\n"
    "ds_read_b128 v[64:67], v4 offset:30720\n"
    "ds_read_b128 v[68:71], v32 offset:0\n"
    "ds_read_b128 v[72:75], v7 offset:2048\n"
    "ds_read_b128 v[76:79], v4 offset:31744\n"
    "ds_read_b128 v[80:83], v32 offset:1024\n"
    "ds_read_b128 v[84:87], v7 offset:3072\n"
    "s_waitcnt lgkmcnt(6)\n"
    "v_add_f32 v88, v88, v89\n"
    "v_add_f32 v90, v90, v91\n"
    "v_add_f32 v88, v88, v90\n"
    "v_rcp_f32 v21, v88\n"
    "s_waitcnt lgkmcnt(5)\n"
    "v_pk_add_f32 v[168:169], v[168:169], v[64:65]\n"
    "s_waitcnt lgkmcnt(3)\n"
    "v_pk_add_f32 v[68:69], v[68:69], v[72:73]\n"
    "v_cmp_nle_f32 vcc, s95, v88\n"
    "v_pk_add_f32 v[170:171], v[170:171], v[66:67]\n"
    "v_pk_add_f32 v[70:71], v[70:71], v[74:75]\n"
    "v_pk_add_f32 v[168:169], v[168:169], v[68:69]\n"
    "v_pk_add_f32 v[170:171], v[170:171], v[70:71]\n"
    "v_pk_mul_f32 v[168:169], v[168:169], v[20:21] op_sel:[0,1] op_sel_hi:[1,1]\n"
    "s_or_b64 s[92:93], s[92:93], vcc\n"
    "v_pk_mul_f32 v[170:171], v[170:171], v[20:21] op_sel:[0,1] op_sel_hi:[1,1]\n"
    "v_cmp_nge_f32 vcc, s96, v88\n"
    "s_waitcnt lgkmcnt(2)\n"
    "v_pk_add_f32 v[172:173], v[172:173], v[76:77]\n"
    "s_waitcnt lgkmcnt(0)\n"
    "v_pk_add_f32 v[80:81], v[80:81], v[84:85]\n"
    "v_pk_add_f32 v[174:175], v[174:175], v[78:79]\n"
    "v_pk_add_f32 v[82:83], v[82:83], v[86:87]\n"
    "v_pk_add_f32 v[172:173], v[172:173], v[80:81]\n"
    "s_or_b64 s[92:93], s[92:93], vcc\n"
    "v_pk_add_f32 v[174:175], v[174:175], v[82:83]\n"
    "v_pk_mul_f32 v[172:173], v[172:173], v[20:21] op_sel:[0,1] op_sel_hi:[1,1]\n"
    "v_pk_mul_f32 v[174:175], v[174:175], v[20:21] op_sel:[0,1] op_sel_hi:[1,1]\n"
    "v_cvt_pk_bf16_f32 v64, v168, v169\n"
    "v_cvt_pk_bf16_f32 v65, v170, v171\n"
    "v_cvt_pk_bf16_f32 v66, v172, v173\n"
    "v_cvt_pk_bf16_f32 v67, v174, v175\n"
    "s_nop 0\n"
    "v_permlane16_swap_b32 v64, v66\n"
    "v_permlane16_swap_b32 v65, v67\n"
    "buffer_store_dwordx4 v[64:67], v20, s[52:55], s61 offen nt\n"
    "s_nop 0\n"
    "v_mfma_f32_16x16x32_bf16 a[244:247], a[240:243], v[64:67], a[244:247]\n"
    "s_nop 7\n"
    "v_accvgpr_read_b32 v28, a244\n"
    "v_cmp_u_f32 vcc, v28, v28\n"
    "s_nop 3\n"
    "s_or_b64 s[92:93], s[92:93], vcc\n"
    "s_cmp_lg_u64 s[92:93], 0\n"
    "s_cselect_b32 %[bad], 1, 0\n"
    : [bad] "=&s"(bad)
    : [tid] "v"(tid), [bx] "s"(bx), [lds] "s"(ldsb),
      [ka] "s"(ka)
    : "v4", "v5", "v6", "v7", "v8", "v9", "v10", "v11", "v12", "v13", "v14", "v15", "v16", "v17", "v18", "v19", "v20", "v21", "v22", "v23", "v24", "v25", "v26", "v27", "v28", "v29", "v30", "v31", "v32", "v33", "v34", "v35", "v36", "v37", "v38", "v39", "v40", "v41", "v42", "v43", "v44", "v45", "v46", "v47", "v48", "v49", "v50", "v51", "v52", "v53", "v54", "v55", "v56", "v57", "v58", "v59", "v60", "v61", "v62", "v63", "v64", "v65", "v66", "v67", "v68", "v69", "v70", "v71", "v72", "v73", "v74", "v75", "v76", "v77", "v78", "v79", "v80", "v81", "v82", "v83", "v84", "v85", "v86", "v87", "v88", "v89", "v90", "v91", "v92", "v93", "v94", "v95", "v96", "v97", "v98", "v99", "v100", "v101", "v102", "v103", "v104", "v105", "v106", "v107", "v108", "v109", "v110", "v111", "v112", "v113", "v114", "v115", "v116", "v117", "v118", "v119", "v120", "v121", "v122", "v123", "v124", "v125", "v126", "v127", "v128", "v129", "v130", "v131", "v132", "v133", "v134", "v135", "v136", "v137", "v138", "v139", "v140", "v141", "v142", "v143", "v144", "v145", "v146", "v147", "v148", "v149", "v150", "v151", "v152", "v153", "v154", "v155", "v156", "v157", "v158", "v159", "v160", "v161", "v162", "v163", "v164", "v165", "v166", "v167", "v168", "v169", "v170", "v171", "v172", "v173", "v174", "v175", "v176", "v177", "v178", "v179", "v180", "v181", "v182", "v183", "v184", "v185", "v186", "v187", "v188", "v189", "v190", "v191", "v192", "v193", "v194", "v195", "v196", "v197", "v198", "v199", "v200", "v201", "v202", "v203", "v204", "v205", "v206", "v207", "v208", "v209", "v210", "v211", "v212", "v213", "v214", "v215", "v216", "v217", "v218", "v219", "v220", "v221", "v222", "v223", "v224", "v225", "v226", "v227", "v228", "v229", "v230", "v231", "v232", "v233", "v234", "v235", "v236", "v237", "v238", "v239", "v240", "v241", "v242", "v243", "v244", "v245", "v246", "v247", "v248", "v249", "v250", "v251", "v252", "v253", "v254", "v255", "a0", "a1", "a2", "a3", "a4", "a5", "a6", "a7", "a8", "a9", "a10", "a11", "a12", "a13", "a14", "a15", "a16", "a17", "a18", "a19", "a20", "a21", "a22", "a23", "a24", "a25", "a26", "a27", "a28", "a29", "a30", "a31", "a32", "a33", "a34", "a35", "a36", "a37", "a38", "a39", "a40", "a41", "a42", "a43", "a44", "a45", "a46", "a47", "a48", "a49", "a50", "a51", "a52", "a53", "a54", "a55", "a56", "a57", "a58", "a59", "a60", "a61", "a62", "a63", "a64", "a65", "a66", "a67", "a68", "a69", "a70", "a71", "a72", "a73", "a74", "a75", "a76", "a77", "a78", "a79", "a80", "a81", "a82", "a83", "a84", "a85", "a86", "a87", "a88", "a89", "a90", "a91", "a92", "a93", "a94", "a95", "a96", "a97", "a98", "a99", "a100", "a101", "a102", "a103", "a104", "a105", "a106", "a107", "a108", "a109", "a110", "a111", "a112", "a113", "a114", "a115", "a116", "a117", "a118", "a119", "a120", "a121", "a122", "a123", "a124", "a125", "a126", "a127", "a128", "a129", "a130", "a131", "a132", "a133", "a134", "a135", "a136", "a137", "a138", "a139", "a140", "a141", "a142", "a143", "a144", "a145", "a146", "a147", "a148", "a149", "a150", "a151", "a152", "a153", "a154", "a155", "a156", "a157", "a158", "a159", "a160", "a161", "a162", "a163", "a164", "a165", "a166", "a167", "a168", "a169", "a170", "a171", "a172", "a173", "a174", "a175", "a176", "a177", "a178", "a179", "a180", "a181", "a182", "a183", "a184", "a185", "a186", "a187", "a188", "a189", "a190", "a191", "a192", "a193", "a194", "a195", "a196", "a197", "a198", "a199", "a200", "a201", "a202", "a203", "a204", "a205", "a206", "a207", "a208", "a209", "a210", "a211", "a212", "a213", "a214", "a215", "a216", "a217", "a218", "a219", "a220", "a221", "a222", "a223", "a224", "a225", "a226", "a227", "a228", "a229", "a230", "a231", "a232", "a233", "a234", "a235", "a236", "a237", "a238", "a239", "a240", "a241", "a242", "a243", "a244", "a245", "a246", "a247", "a248", "a249", "a250", "a251", "a252", "a253", "a254", "a255", "s36", "s37", "s38", "s39", "s40", "s41", "s42", "s43", "s44", "s45", "s46", "s47", "s48", "s49", "s50", "s51", "s52", "s53", "s54", "s55", "s56", "s57", "s58", "s59", "s60", "s61", "s62", "s63", "s64", "s65", "s66", "s67", "s68", "s69", "s70", "s71", "s72", "s73", "s74", "s75", "s76", "s77", "s78", "s79", "s80", "s81", "s82", "s83", "s84", "s85", "s86", "s87", "s88", "s89", "s90", "s91", "s92", "s93", "s94", "s95", "s96", "s97", "s98", "s99", "vcc", "scc", "memory");
  if (bad) {
    asm volatile("s_waitcnt vmcnt(0)" ::: "memory");
    slow_path(Q, K, V, O, (bx & 7) + 8 * (bx >> 5), (bx >> 3) & 3, tid >> 6, tid & 63);
  }
}

extern "C" void kernel_launch(void* const* d_in, const int* in_sizes, int n_in, void* d_out, int out_size, void* d_ws, size_t ws_size,
                              hipStream_t stream) {
  gqa_attn_kernel<<<256, 256, 0, stream>>>((const u16*)d_in[0], (const u16*)d_in[1], (const u16*)d_in[2], (u16*)d_out);
}
```
